```python
import torch

def _softmax(x, axis=-1):
    return torch.softmax(x, dim=axis)

def _gelu(x):
    return torch.nn.functional.gelu(x, approximate='tanh')

def _silu(x):
    return torch.nn.functional.silu(x)

def _sigmoid(x):
    return torch.sigmoid(x)

def _layernorm(x, axis=-1, eps=1e-05):
    m = x.mean(dim=axis, keepdim=True)
    v = x.var(dim=axis, keepdim=True, unbiased=False)
    return (x - m) / torch.sqrt(v + eps)

def _scatter_add(tgt, idx, src, axis=0):
    return tgt.clone().index_add_(axis, idx, src)

def _segment_sum(src, seg, n):
    out = torch.zeros((n,) + src.shape[1:], dtype=src.dtype)
    return out.index_add_(0, seg, src)

def _pad(x, pw):
    flat = tuple((v for pair in reversed(pw) for v in pair))
    return torch.nn.functional.pad(x, flat)
kernel_name = 'standalone__swin_window_attn__vision'

def setup_inputs():

    _w = lambda a: (lambda t: t.to(torch.bfloat16) if t.is_floating_point() else t)(torch.as_tensor(a))
    H = W = 384 // 8 * 8
    C, win = (192, 8)
    return {'x': _w((lambda *s: torch.randn(*s, dtype=torch.bfloat16) * 0.2)(1, H, W, C)), 'wqkv': _w((lambda *s: torch.randn(*s, dtype=torch.bfloat16) * 0.2)(C, 3 * C)), 'wo': _w((lambda *s: torch.randn(*s, dtype=torch.bfloat16) * 0.05)(C, C))}

def reference(x, wqkv, wo):
    B, H, W, C = x.shape
    win = 8
    nh, nw = (H // win, W // win)
    xw = (lambda x, *d: x.permute(*d))(x.reshape(B, nh, win, nw, win, C), 0, 1, 3, 2, 4, 5)
    xw = xw.reshape(B * nh * nw, win * win, C)
    qkv = xw @ wqkv
    q, k, v = (qkv[..., :C].clone(), qkv[..., C:2 * C].clone(), qkv[..., 2 * C:].clone())
    scale = C ** (-0.5)
    p = _softmax(torch.einsum('bqc,bkc->bqk', q, k) * scale, axis=-1)
    o = torch.einsum('bqk,bkc->bqc', p, v) @ wo
    o = (lambda x, *d: x.permute(*d))(o.reshape(B, nh, nw, win, win, C), 0, 1, 3, 2, 4, 5)
    return o.reshape(B, H, W, C)
```

```cpp
#include <hip/hip_runtime.h>
#include <hip/hip_bf16.h>
#include <stdint.h>

typedef float    f32x16 __attribute__((ext_vector_type(16)));
typedef float    f32x2  __attribute__((ext_vector_type(2)));
typedef __bf16   bf16x8 __attribute__((ext_vector_type(8)));
typedef __bf16   bf16x2 __attribute__((ext_vector_type(2)));
typedef unsigned u32x4  __attribute__((ext_vector_type(4)));
#define DI __device__ __forceinline__
#define MFMA(a, b, c) __builtin_amdgcn_mfma_f32_32x32x16_bf16((a), (b), (c), 0, 0, 0)

constexpr int C = 192, HW = 384, NWIN = 48 * 48;
constexpr int FRAG_BYTES = 1024;
constexpr int WMAT_BYTES = 6 * 12 * FRAG_BYTES;

DI unsigned cvtpk(float lo, float hi) {
  f32x2 v = {lo, hi};
  bf16x2 b = __builtin_convertvector(v, bf16x2);
  return __builtin_bit_cast(unsigned, b);
}
DI bf16x8 pack8(const f32x16& x, int s) {
  u32x4 p;
  p[0] = cvtpk(x[8 * s + 0], x[8 * s + 1]);
  p[1] = cvtpk(x[8 * s + 2], x[8 * s + 3]);
  p[2] = cvtpk(x[8 * s + 4], x[8 * s + 5]);
  p[3] = cvtpk(x[8 * s + 6], x[8 * s + 7]);
  return __builtin_bit_cast(bf16x8, p);
}
DI int perm23(int r) { return (r & 0x13) | ((r & 4) << 1) | ((r & 8) >> 1); }

__global__ __launch_bounds__(64) void prep_kernel(const unsigned short* __restrict__ wqkv, const unsigned short* __restrict__ wo,
                                                  u32x4* __restrict__ wfr) {
  const int lane = threadIdx.x, r = lane & 31, h = lane >> 5;
  const int id = blockIdx.x;
  const int mat = id / 36, u = (id % 36) / 6, t = id % 6;
  const int pr = perm23(r);
  f32x16 acc;
  for (int i = 0; i < 16; ++i) acc[i] = 0.f;
  if (mat == 0) {
    const unsigned short* arow = wqkv + (size_t)(32 * u + pr) * 576 + 8 * h;
    const unsigned short* brow = wqkv + (size_t)(32 * t + pr) * 576 + 192 + 8 * h;
#pragma unroll
    for (int ks = 0; ks < 12; ++ks) {
      bf16x8 a = *(const bf16x8*)(arow + 16 * ks);
      bf16x8 b = *(const bf16x8*)(brow + 16 * ks);
      acc = MFMA(a, b, acc);
    }
    const float sc = 0.07216878364870322f * 1.4426950408889634f;
    for (int i = 0; i < 16; ++i) acc[i] *= sc;
  } else {
    const unsigned short* arow = wqkv + (size_t)(32 * u + pr) * 576 + 384 + 8 * h;
    const unsigned short* bcol = wo + (size_t)(8 * h) * 192 + 32 * t + pr;
#pragma unroll
    for (int ks = 0; ks < 12; ++ks) {
      bf16x8 a = *(const bf16x8*)(arow + 16 * ks);
      unsigned short tmp[8];
#pragma unroll
      for (int j = 0; j < 8; ++j) tmp[j] = bcol[(size_t)(16 * ks + j) * 192];
      u32x4 p;
      p[0] = tmp[0] | ((unsigned)tmp[1] << 16);
      p[1] = tmp[2] | ((unsigned)tmp[3] << 16);
      p[2] = tmp[4] | ((unsigned)tmp[5] << 16);
      p[3] = tmp[6] | ((unsigned)tmp[7] << 16);
      acc = MFMA(a, __builtin_bit_cast(bf16x8, p), acc);
    }
  }
#pragma unroll
  for (int s = 0; s < 2; ++s) {
    bf16x8 f = pack8(acc, s);
    wfr[(size_t)mat * (WMAT_BYTES / 16) + ((t * 12 + 2 * u + s) * 64) + lane] = __builtin_bit_cast(u32x4, f);
  }
}

DI float xhalf_max(float v) {
  return fmaxf(v, __shfl_xor(v, 32));
}
DI float xhalf_sum(float v) { return v + __shfl_xor(v, 32); }

DI void process_window(const unsigned short* __restrict__ x, unsigned short* __restrict__ out, int win, const char* lds, int lane) {
  const int r = lane & 31, h = lane >> 5;
  const int wy = win / 48, wx = win % 48;
  size_t tokoff[2];
#pragma unroll
  for (int tt = 0; tt < 2; ++tt) {
    int t = 32 * tt + r;
    tokoff[tt] = ((size_t)(8 * wy + (t >> 3)) * HW + 8 * wx + (t & 7)) * C + 8 * h;
  }
  bf16x8 xf[2][12];
#pragma unroll
  for (int tt = 0; tt < 2; ++tt)
#pragma unroll
    for (int ks = 0; ks < 12; ++ks) xf[tt][ks] = *(const bf16x8*)(x + tokoff[tt] + 16 * ks);

  const char* mfr = lds + lane * 16;
  const char* nfr = lds + WMAT_BYTES + lane * 16;
  f32x16 zero;
#pragma unroll
  for (int i = 0; i < 16; ++i) zero[i] = 0.f;
  f32x16 s[2][2];
#pragma unroll
  for (int a = 0; a < 2; ++a)
#pragma unroll
    for (int b = 0; b < 2; ++b) s[a][b] = zero;
#pragma unroll
  for (int ct = 0; ct < 6; ++ct) {
    f32x16 t0 = zero, t1 = zero;
#pragma unroll
    for (int ks = 0; ks < 12; ++ks) {
      bf16x8 m = *(const bf16x8*)(mfr + (ct * 12 + ks) * FRAG_BYTES);
      t0 = MFMA(m, xf[0][ks], t0);
      t1 = MFMA(m, xf[1][ks], t1);
    }
#pragma unroll
    for (int sp = 0; sp < 2; ++sp) {
      bf16x8 p0 = pack8(t0, sp), p1 = pack8(t1, sp);
      s[0][0] = MFMA(xf[0][2 * ct + sp], p0, s[0][0]);
      s[1][0] = MFMA(xf[1][2 * ct + sp], p0, s[1][0]);
      s[0][1] = MFMA(xf[0][2 * ct + sp], p1, s[0][1]);
      s[1][1] = MFMA(xf[1][2 * ct + sp], p1, s[1][1]);
    }
  }
  bf16x8 pf[2][4];
#pragma unroll
  for (int qt = 0; qt < 2; ++qt) {
    float m = s[0][qt][0];
#pragma unroll
    for (int i = 1; i < 16; ++i) m = fmaxf(m, s[0][qt][i]);
#pragma unroll
    for (int i = 0; i < 16; ++i) m = fmaxf(m, s[1][qt][i]);
    m = xhalf_max(m);
    float l = 0.f;
#pragma unroll
    for (int kt = 0; kt < 2; ++kt)
#pragma unroll
      for (int i = 0; i < 16; ++i) {
        float p = __builtin_amdgcn_exp2f(s[kt][qt][i] - m);
        s[kt][qt][i] = p;
        l += p;
      }
    l = xhalf_sum(l);
    float inv = 1.0f / l;
#pragma unroll
    for (int kt = 0; kt < 2; ++kt) {
#pragma unroll
      for (int i = 0; i < 16; ++i) s[kt][qt][i] *= inv;
      pf[qt][2 * kt + 0] = pack8(s[kt][qt], 0);
      pf[qt][2 * kt + 1] = pack8(s[kt][qt], 1);
    }
  }
#pragma unroll
  for (int ct = 0; ct < 6; ++ct) {
    f32x16 u0 = zero, u1 = zero;
#pragma unroll
    for (int ks = 0; ks < 12; ++ks) {
      bf16x8 n = *(const bf16x8*)(nfr + (ct * 12 + ks) * FRAG_BYTES);
      u0 = MFMA(xf[0][ks], n, u0);
      u1 = MFMA(xf[1][ks], n, u1);
    }
    bf16x8 uf[4];
    uf[0] = pack8(u0, 0); uf[1] = pack8(u0, 1); uf[2] = pack8(u1, 0); uf[3] = pack8(u1, 1);
#pragma unroll
    for (int qt = 0; qt < 2; ++qt) {
      f32x16 o = zero;
#pragma unroll
      for (int k = 0; k < 4; ++k) o = MFMA(uf[k], pf[qt][k], o);
#pragma unroll
      for (int sp = 0; sp < 2; ++sp) {
        bf16x8 ov = pack8(o, sp);
        *(bf16x8*)(out + tokoff[qt] + 32 * ct + 16 * sp) = ov;
      }
    }
  }
}

__global__ __launch_bounds__(512, 2) void swin_main(const unsigned short* __restrict__ x, const u32x4* __restrict__ wfr,
                                                    unsigned short* __restrict__ out) {
  extern __shared__ __attribute__((aligned(16))) char lds[];
  const int tid = threadIdx.x, lane = tid & 63;
  const int wave = __builtin_amdgcn_readfirstlane(tid >> 6);
#pragma unroll
  for (int i = 0; i < 18; ++i) ((u32x4*)lds)[i * 512 + tid] = wfr[i * 512 + tid];
  __syncthreads();
  const int base = blockIdx.x * 9;
  const int nw = wave == 0 ? 2 : 1;
#pragma unroll 1
  for (int it = 0; it < nw; ++it) {
    process_window(x, out, base + wave + 8 * it, lds, lane);
    asm volatile("" ::: "memory");
  }
}

extern "C" void kernel_launch(void* const* d_in, const int* in_sizes, int n_in,
                              void* d_out, int out_size, void* d_ws, size_t ws_size,
                              hipStream_t stream) {
  const unsigned short* x = (const unsigned short*)d_in[0];
  const unsigned short* wqkv = (const unsigned short*)d_in[1];
  const unsigned short* wo = (const unsigned short*)d_in[2];
  u32x4* wfr = (u32x4*)d_ws;
  (void)hipFuncSetAttribute((const void*)swin_main, hipFuncAttributeMaxDynamicSharedMemorySize, 2 * WMAT_BYTES);
  prep_kernel<<<72, 64, 0, stream>>>(wqkv, wo, wfr);
  swin_main<<<256, 512, 2 * WMAT_BYTES, stream>>>(x, wfr, (unsigned short*)d_out);
}
```

```cpp
#include <hip/hip_runtime.h>
#include <stdint.h>

constexpr unsigned LDS_BYTES = 163840;

__global__ __launch_bounds__(256, 1) void swin_fused(const void* __restrict__ xg, const void* __restrict__ wqkv,
                                                     const void* __restrict__ wo, void* __restrict__ outg, void* __restrict__ ws) {
  asm volatile(
    ".Lw_prep0:\n"
    "s_load_dwordx8 s[4:11], s[0:1], 0x0\n"
    "s_load_dwordx2 s[14:15], s[0:1], 0x20\n"
    "s_getpc_b64 s[78:79]\n"
    ".Lswin_pc:\n"
    "s_lshl_b32 s80, s2, 7\n"
    "s_and_b32 s80, s80, 0xfffffc00\n"
    "s_min_u32 s80, s80, .Lswin_end-.Lswin_pc-1024\n"
    "v_lshlrev_b32 v201, 2, v0\n"
    "s_add_u32 s78, s78, s80\n"
    "s_addc_u32 s79, s79, 0\n"
    "global_load_dword v224, v201, s[78:79]\n"
    "v_readfirstlane_b32 s12, v0\n"
    "v_and_b32 v1, 63, v0\n"
    "s_lshr_b32 s66, s2, 3\n"
    "v_lshlrev_b32 v17, 4, v1\n"
    "v_lshrrev_b32 v2, 3, v1\n"
    "v_and_b32 v3, 3, v2\n"
    "v_and_b32 v4, 4, v2\n"
    "s_lshr_b32 s12, s12, 6\n"
    "v_lshl_or_b32 v3, v4, 1, v3\n"
    "s_and_b32 s13, s12, 1\n"
    "s_lshr_b32 s69, s12, 1\n"
    "s_mov_b32 s86, 172032\n"
    "s_mov_b32 s87, 0x20000\n"
    "s_cmp_ge_u32 s12, 2\n"
    "s_cbranch_scc1 .Lw_cons\n"
    "v_mul_u32_u24 v3, 0x480, v3\n"
    "v_and_b32 v4, 7, v1\n"
    "v_xor_b32 v4, v4, v2\n"
    "v_lshl_add_u32 v231, v4, 4, v3\n"
    "v_mul_u32_u24 v18, 0x180, v2\n"
    "v_lshl_add_u32 v18, v4, 4, v18\n"
    "s_mov_b32 s30, 221184\n"
    "s_mov_b32 s31, 0x20000\n"
    "s_mov_b32 s34, 73728\n"
    "s_mov_b32 s35, 0x20000\n"
    "s_mul_i32 s18, s2, 57\n"
    "s_lshr_b32 s18, s18, 12\n"
    "s_mul_i32 s18, s18, 69\n"
    "s_sub_u32 s18, s2, s18\n"
    "s_sub_u32 s19, s18, 72\n"
    "s_cmp_ge_u32 s18, 72\n"
    "s_cselect_b32 s18, s19, s18\n"
    "s_cmp_ge_u32 s18, 36\n"
    "s_cselect_b32 s36, 1, 0\n"
    "s_mul_i32 s37, s36, 36\n"
    "s_sub_u32 s37, s18, s37\n"
    "s_mul_i32 s38, s37, 43\n"
    "s_lshr_b32 s38, s38, 8\n"
    "s_mul_i32 s39, s38, 6\n"
    "s_sub_u32 s39, s37, s39\n"
    "s_lshl_b32 s40, s13, 2\n"
    "s_lshl_b32 s41, s38, 5\n"
    "s_add_u32 s41, s41, s40\n"
    "s_mul_i32 s41, s41, 1152\n"
    "s_mul_i32 s42, s36, 768\n"
    "s_add_u32 s41, s41, s42\n"
    "s_lshl_b32 s42, s39, 5\n"
    "s_add_u32 s42, s42, s40\n"
    "s_mul_i32 s42, s42, 1152\n"
    "s_add_u32 s42, s42, 384\n"
    "s_lshr_b32 s43, s39, 1\n"
    "s_lshl_b32 s43, s43, 7\n"
    "s_mul_i32 s44, s13, 36864\n"
    "s_add_u32 s43, s43, s44\n"
    "s_lshl_b32 s45, s13, 11\n"
    "s_add_u32 s45, s45, 73728\n"
    "s_mul_i32 s46, s13, 12288\n"
    "s_add_u32 s46, s46, 86016\n"
    "v_mov_b32 v19, 0\n"
    "s_waitcnt lgkmcnt(0)\n"
    "s_mov_b32 s84, s14\n"
    "s_and_b32 s85, s15, 0xffff\n"
    "s_mov_b32 s28, s6\n"
    "s_and_b32 s29, s7, 0xffff\n"
    "s_mov_b32 s32, s8\n"
    "s_and_b32 s33, s9, 0xffff\n"
    "buffer_load_dwordx2 v[20:21], v19, s[84:87], 0 offen sc1\n"
    "s_cmp_ge_u32 s2, 72\n"
    "s_cbranch_scc1 .Lw_cons\n"
    "s_add_u32 m0, s45, 0\n"
    "s_add_u32 s47, s41, 0\n"
    "buffer_load_dwordx4 v231, s[28:31], s47 offen lds\n"
    "s_add_u32 m0, s45, 1024\n"
    "s_add_u32 s47, s41, 18432\n"
    "buffer_load_dwordx4 v231, s[28:31], s47 offen lds\n"
    "s_add_u32 m0, s45, 4096\n"
    "s_add_u32 s47, s41, 128\n"
    "buffer_load_dwordx4 v231, s[28:31], s47 offen lds\n"
    "s_add_u32 m0, s45, 5120\n"
    "s_add_u32 s47, s41, 18560\n"
    "buffer_load_dwordx4 v231, s[28:31], s47 offen lds\n"
    "s_add_u32 m0, s45, 8192\n"
    "s_add_u32 s47, s41, 256\n"
    "buffer_load_dwordx4 v231, s[28:31], s47 offen lds\n"
    "s_add_u32 m0, s45, 9216\n"
    "s_add_u32 s47, s41, 18688\n"
    "buffer_load_dwordx4 v231, s[28:31], s47 offen lds\n"
    "s_cmp_eq_u32 s36, 1\n"
    "s_cbranch_scc1 .Lw_ldN\n"
    "s_add_u32 m0, s45, 12288\n"
    "s_add_u32 s47, s42, 0\n"
    "buffer_load_dwordx4 v231, s[28:31], s47 offen lds\n"
    "s_add_u32 m0, s45, 13312\n"
    "s_add_u32 s47, s42, 18432\n"
    "buffer_load_dwordx4 v231, s[28:31], s47 offen lds\n"
    "s_add_u32 m0, s45, 16384\n"
    "s_add_u32 s47, s42, 128\n"
    "buffer_load_dwordx4 v231, s[28:31], s47 offen lds\n"
    "s_add_u32 m0, s45, 17408\n"
    "s_add_u32 s47, s42, 18560\n"
    "buffer_load_dwordx4 v231, s[28:31], s47 offen lds\n"
    "s_add_u32 m0, s45, 20480\n"
    "s_add_u32 s47, s42, 256\n"
    "buffer_load_dwordx4 v231, s[28:31], s47 offen lds\n"
    "s_add_u32 m0, s45, 21504\n"
    "s_add_u32 s47, s42, 18688\n"
    "buffer_load_dwordx4 v231, s[28:31], s47 offen lds\n"
    "s_branch .Lw_cons\n"
    ".Lw_ldN:\n"
    "s_add_u32 m0, s46, 0\n"
    "s_add_u32 s47, s43, 0\n"
    "buffer_load_dwordx4 v18, s[32:35], s47 offen lds\n"
    "s_add_u32 m0, s46, 1024\n"
    "s_add_u32 s47, s43, 3072\n"
    "buffer_load_dwordx4 v18, s[32:35], s47 offen lds\n"
    "s_add_u32 m0, s46, 2048\n"
    "s_add_u32 s47, s43, 6144\n"
    "buffer_load_dwordx4 v18, s[32:35], s47 offen lds\n"
    "s_add_u32 m0, s46, 3072\n"
    "s_add_u32 s47, s43, 9216\n"
    "buffer_load_dwordx4 v18, s[32:35], s47 offen lds\n"
    "s_add_u32 m0, s46, 4096\n"
    "s_add_u32 s47, s43, 12288\n"
    "buffer_load_dwordx4 v18, s[32:35], s47 offen lds\n"
    "s_add_u32 m0, s46, 5120\n"
    "s_add_u32 s47, s43, 15360\n"
    "buffer_load_dwordx4 v18, s[32:35], s47 offen lds\n"
    "s_add_u32 m0, s46, 6144\n"
    "s_add_u32 s47, s43, 18432\n"
    "buffer_load_dwordx4 v18, s[32:35], s47 offen lds\n"
    "s_add_u32 m0, s46, 7168\n"
    "s_add_u32 s47, s43, 21504\n"
    "buffer_load_dwordx4 v18, s[32:35], s47 offen lds\n"
    "s_add_u32 m0, s46, 8192\n"
    "s_add_u32 s47, s43, 24576\n"
    "buffer_load_dwordx4 v18, s[32:35], s47 offen lds\n"
    "s_add_u32 m0, s46, 9216\n"
    "s_add_u32 s47, s43, 27648\n"
    "buffer_load_dwordx4 v18, s[32:35], s47 offen lds\n"
    "s_add_u32 m0, s46, 10240\n"
    "s_add_u32 s47, s43, 30720\n"
    "buffer_load_dwordx4 v18, s[32:35], s47 offen lds\n"
    "s_add_u32 m0, s46, 11264\n"
    "s_add_u32 s47, s43, 33792\n"
    "buffer_load_dwordx4 v18, s[32:35], s47 offen lds\n"
    ".Lw_cons:\n"
    "v_mul_u32_u24 v5, 43, v1\n"
    "v_or_b32 v6, 64, v1\n"
    "v_or_b32 v7, 0x80, v1\n"
    "v_lshrrev_b32 v5, 10, v5\n"
    "v_mul_u32_u24 v6, 43, v6\n"
    "v_mul_u32_u24 v7, 0xab, v7\n"
    "v_lshrrev_b32 v6, 10, v6\n"
    "v_lshrrev_b32 v7, 12, v7\n"
    "s_cmp_ge_u32 s12, 2\n"
    "s_cselect_b64 vcc, -1, 0\n"
    "s_mov_b32 s22, 56623104\n"
    "s_mov_b32 s23, 0x20000\n"
    "s_mov_b32 s26, 56623104\n"
    "s_mov_b32 s27, 0x20000\n"
    "v_xor_b32 v8, v5, v1\n"
    "v_lshlrev_b32 v228, 4, v8\n"
    "v_xor_b32 v8, v6, v1\n"
    "v_lshlrev_b32 v229, 4, v8\n"
    "v_xor_b32 v8, v7, v1\n"
    "v_lshlrev_b32 v230, 4, v8\n"
    "v_add_u32 v245, 4608, v231\n"
    "v_and_b32 v2, 15, v1\n"
    "v_lshrrev_b32 v3, 4, v1\n"
    "v_and_b32 v4, 7, v1\n"
    "v_xor_b32 v5, v3, v4\n"
    "v_lshlrev_b32 v5, 4, v5\n"
    "v_lshl_add_u32 v6, v2, 7, v5\n"
    "v_mul_u32_u24 v7, 0x180, v2\n"
    "v_add_u32 v7, v7, v5\n"
    "v_lshlrev_b32 v9, 4, v1\n"
    "s_lshl_b32 s70, s13, 6\n"
    "v_xor_b32 v10, s70, v7\n"
    "v_xor_b32 v11, 64, v10\n"
    "v_xor_b32 v12, 64, v7\n"
    "s_lshl_b32 s70, s13, 11\n"
    "s_sub_u32 s71, 0, s70\n"
    "s_cmp_ge_u32 s12, 2\n"
    "s_cselect_b32 s70, 12288, s70\n"
    "s_cselect_b32 s71, 12288, s71\n"
    "v_add_u32 v232, s70, v6\n"
    "v_add_u32 v233, s71, v6\n"
    "s_mul_i32 s70, s13, 12288\n"
    "v_add_u32 v13, s70, v6\n"
    "v_lshrrev_b32 v14, 2, v2\n"
    "v_lshl_add_u32 v15, v3, 3, v14\n"
    "v_mul_u32_u24 v15, 0x180, v15\n"
    "v_and_b32 v16, 3, v2\n"
    "v_and_b32 v14, 2, v14\n"
    "v_lshlrev_b32 v14, 1, v14\n"
    "v_xor_b32 v16, v16, v14\n"
    "s_lshl_b32 s71, s13, 2\n"
    "v_xor_b32 v16, s71, v16\n"
    "v_lshl_add_u32 v15, v16, 4, v15\n"
    "v_add_u32 v15, 24576, v15\n"
    "v_cndmask_b32 v234, v13, v15, vcc\n"
    "s_sub_u32 s71, 12288, s70\n"
    "v_add_u32 v13, s70, v10\n"
    "v_cndmask_b32 v235, v13, v7, vcc\n"
    "v_add_u32 v13, s70, v11\n"
    "v_cndmask_b32 v236, v13, v12, vcc\n"
    "v_add_u32 v13, s71, v10\n"
    "v_cndmask_b32 v237, v13, v10, vcc\n"
    "v_add_u32 v13, s71, v11\n"
    "v_add_u32 v14, 147456, v9\n"
    "v_cndmask_b32 v238, v13, v14, vcc\n"
    "v_add_u32 v13, 12288, v7\n"
    "v_cndmask_b32 v254, v237, v13, vcc\n"
    "v_add_u32 v13, 12288, v12\n"
    "v_cndmask_b32 v255, v238, v13, vcc\n"
    "s_mul_i32 s70, s13, 3072\n"
    "s_mul_i32 s71, s12, 3072\n"
    "s_mul_i32 s72, s13, 6144\n"
    "s_add_u32 s73, s72, 122880\n"
    "s_sub_u32 s74, 129024, s72\n"
    "s_add_u32 s75, s71, 98304\n"
    "s_cmp_ge_u32 s12, 2\n"
    "s_cselect_b32 s73, s70, s73\n"
    "s_cselect_b32 s74, s75, s74\n"
    "s_cselect_b32 s56, s70, 0x100000\n"
    "v_add_u32 v239, s73, v9\n"
    "v_add_u32 v240, s74, v9\n"
    "v_add_u32 v244, s75, v9\n"
    "s_mul_i32 s72, s13, 5120\n"
    "v_add_u32 v241, s72, v14\n"
    "s_mul_i32 s72, s13, 3072\n"
    "s_add_u32 s72, s72, 1024\n"
    "v_add_u32 v242, s72, v14\n"
    "v_add_u32 v243, s70, v9\n"
    "s_add_u32 s70, s2, 0\n"
    "s_mul_hi_u32 s52, s70, 0xaaaaaaab\n"
    "s_lshr_b32 s52, s52, 5\n"
    "s_mul_i32 s53, s52, 48\n"
    "s_sub_u32 s53, s70, s53\n"
    "s_mul_i32 s52, s52, 1179648\n"
    "s_mul_i32 s70, s53, 3072\n"
    "s_add_u32 s52, s52, s70\n"
    "s_mul_i32 s71, s13, 147456\n"
    "s_add_u32 s54, s52, s71\n"
    "s_mov_b32 s55, s53\n"
    "s_add_u32 s70, s2, 2048\n"
    "s_mul_hi_u32 s57, s70, 0xaaaaaaab\n"
    "s_lshr_b32 s57, s57, 5\n"
    "s_mul_i32 s72, s57, 48\n"
    "s_sub_u32 s72, s70, s72\n"
    "s_mul_i32 s57, s57, 1179648\n"
    "s_mul_i32 s70, s72, 3072\n"
    "s_add_u32 s57, s57, s70\n"
    "s_mul_i32 s71, s12, 147456\n"
    "s_add_u32 s57, s57, s71\n"
    "s_cmp_ge_u32 s12, 2\n"
    "s_cselect_b32 s52, s54, 56623104\n"
    "s_waitcnt lgkmcnt(0)\n"
    "s_mov_b32 s84, s14\n"
    "s_and_b32 s85, s15, 0xffff\n"
    "s_mov_b32 s20, s4\n"
    "s_and_b32 s21, s5, 0xffff\n"
    "s_mov_b32 s24, s10\n"
    "s_and_b32 s25, s11, 0xffff\n"
    "s_cbranch_vccz .Lw_waveA\n"
    ".Lw_waveB:\n"
    "s_cmp_ge_u32 s2, 72\n"
    "s_cbranch_scc1 .Lw_nb1\n"
    "s_barrier\n"
    "s_barrier\n"
    ".Lw_nb1:\n"
    "s_sleep 16\n"
    "s_add_u32 s74, s56, 0\n"
    "s_mov_b32 m0, s74\n"
    "s_add_u32 s75, s52, 0\n"
    "buffer_load_dwordx4 v228, s[20:23], s75 offen nt lds\n"
    "buffer_load_dwordx4 v229, s[20:23], s75 offen offset:1024 nt lds\n"
    "buffer_load_dwordx4 v230, s[20:23], s75 offen offset:2048 nt lds\n"
    "s_add_u32 s74, s56, 6144\n"
    "s_mov_b32 m0, s74\n"
    "s_add_u32 s75, s52, 294912\n"
    "buffer_load_dwordx4 v228, s[20:23], s75 offen nt lds\n"
    "buffer_load_dwordx4 v229, s[20:23], s75 offen offset:1024 nt lds\n"
    "buffer_load_dwordx4 v230, s[20:23], s75 offen offset:2048 nt lds\n"
    "s_add_u32 s74, s56, 12288\n"
    "s_mov_b32 m0, s74\n"
    "s_add_u32 s75, s52, 589824\n"
    "buffer_load_dwordx4 v228, s[20:23], s75 offen nt lds\n"
    "buffer_load_dwordx4 v229, s[20:23], s75 offen offset:1024 nt lds\n"
    "buffer_load_dwordx4 v230, s[20:23], s75 offen offset:2048 nt lds\n"
    "s_add_u32 s74, s56, 18432\n"
    "s_mov_b32 m0, s74\n"
    "s_add_u32 s75, s52, 884736\n"
    "buffer_load_dwordx4 v228, s[20:23], s75 offen nt lds\n"
    "buffer_load_dwordx4 v229, s[20:23], s75 offen offset:1024 nt lds\n"
    "buffer_load_dwordx4 v230, s[20:23], s75 offen offset:2048 nt lds\n"
    "s_add_u32 s52, s52, 5947392\n"
    "s_add_u32 s53, s53, 16\n"
    "s_cmp_ge_u32 s53, 48\n"
    "s_cselect_b32 s66, 1032192, 0\n"
    "s_cselect_b32 s67, 48, 0\n"
    "s_add_u32 s52, s52, s66\n"
    "s_sub_u32 s53, s53, s67\n"
    "s_add_u32 s74, s56, 24576\n"
    "s_mov_b32 m0, s74\n"
    "s_add_u32 s75, s52, 0\n"
    "buffer_load_dwordx4 v228, s[20:23], s75 offen nt lds\n"
    "buffer_load_dwordx4 v229, s[20:23], s75 offen offset:1024 nt lds\n"
    "buffer_load_dwordx4 v230, s[20:23], s75 offen offset:2048 nt lds\n"
    "s_add_u32 s74, s56, 30720\n"
    "s_mov_b32 m0, s74\n"
    "s_add_u32 s75, s52, 294912\n"
    "buffer_load_dwordx4 v228, s[20:23], s75 offen nt lds\n"
    "buffer_load_dwordx4 v229, s[20:23], s75 offen offset:1024 nt lds\n"
    "buffer_load_dwordx4 v230, s[20:23], s75 offen offset:2048 nt lds\n"
    "s_add_u32 s74, s56, 36864\n"
    "s_mov_b32 m0, s74\n"
    "s_add_u32 s75, s52, 589824\n"
    "buffer_load_dwordx4 v228, s[20:23], s75 offen nt lds\n"
    "buffer_load_dwordx4 v229, s[20:23], s75 offen offset:1024 nt lds\n"
    "buffer_load_dwordx4 v230, s[20:23], s75 offen offset:2048 nt lds\n"
    "s_add_u32 s74, s56, 43008\n"
    "s_mov_b32 m0, s74\n"
    "s_add_u32 s75, s52, 884736\n"
    "buffer_load_dwordx4 v228, s[20:23], s75 offen nt lds\n"
    "buffer_load_dwordx4 v229, s[20:23], s75 offen offset:1024 nt lds\n"
    "buffer_load_dwordx4 v230, s[20:23], s75 offen offset:2048 nt lds\n"
    "s_add_u32 s52, s52, 5947392\n"
    "s_add_u32 s53, s53, 16\n"
    "s_cmp_ge_u32 s53, 48\n"
    "s_cselect_b32 s66, 1032192, 0\n"
    "s_cselect_b32 s67, 48, 0\n"
    "s_add_u32 s52, s52, s66\n"
    "s_sub_u32 s53, s53, s67\n"
    "s_branch .Lw_seen\n"
    ".Lw_waveA:\n"
    "s_waitcnt vmcnt(0)\n"
    "v_readfirstlane_b32 s16, v20\n"
    "v_readfirstlane_b32 s17, v21\n"
    "s_nop 3\n"
    "s_add_u32 s16, s16, 1\n"
    "s_addc_u32 s17, s17, 0\n"
    "v_mov_b32 v20, s16\n"
    "v_mov_b32 v21, s17\n"
    "s_cmp_ge_u32 s2, 72\n"
    "s_cbranch_scc1 .Lw_poll\n"
    "s_barrier\n"
    "v_add_u32 v22, 73728, v6\n"
    "v_xor_b32 v23, 64, v22\n"
    "s_cmp_eq_u32 s36, 1\n"
    "s_cbranch_scc1 .Lw_rdN\n"
    ".Lw_rdM:\n"
    "s_lshl_b32 s47, s13, 11\n"
    "s_add_u32 s47, s47, 86016\n"
    "v_add_u32 v24, s47, v6\n"
    "v_xor_b32 v25, 64, v24\n"
    "ds_read_b128 v[96:99], v24\n"
    "ds_read_b128 v[100:103], v25\n"
    "ds_read_b128 v[104:107], v24 offset:4096\n"
    "ds_read_b128 v[108:111], v25 offset:4096\n"
    "ds_read_b128 v[112:115], v24 offset:8192\n"
    "ds_read_b128 v[116:119], v25 offset:8192\n"
    "s_branch .Lw_mm\n"
    ".Lw_rdN:\n"
    "v_lshrrev_b32 v24, 2, v2\n"
    "v_and_b32 v25, 3, v2\n"
    "s_and_b32 s47, s39, 1\n"
    "s_lshl_b32 s47, s47, 2\n"
    "v_or_b32 v25, s47, v25\n"
    "v_xor_b32 v25, v25, v24\n"
    "v_lshl_add_u32 v24, v3, 3, v24\n"
    "v_lshlrev_b32 v24, 7, v24\n"
    "v_lshl_add_u32 v24, v25, 4, v24\n"
    "s_lshl_b32 s47, s13, 3\n"
    "s_add_u32 s47, s47, 86016\n"
    "v_add_u32 v24, s47, v24\n"
    "v_xor_b32 v25, 64, v24\n"
    "ds_read_b64_tr_b16 v[96:97], v24\n"
    "ds_read_b64_tr_b16 v[98:99], v25 offset:512\n"
    "ds_read_b64_tr_b16 v[100:101], v24 offset:4096\n"
    "ds_read_b64_tr_b16 v[102:103], v25 offset:4608\n"
    "ds_read_b64_tr_b16 v[104:105], v24 offset:8192\n"
    "ds_read_b64_tr_b16 v[106:107], v25 offset:8704\n"
    "ds_read_b64_tr_b16 v[108:109], v24 offset:12288\n"
    "ds_read_b64_tr_b16 v[110:111], v25 offset:12800\n"
    "ds_read_b64_tr_b16 v[112:113], v24 offset:16384\n"
    "ds_read_b64_tr_b16 v[114:115], v25 offset:16896\n"
    "ds_read_b64_tr_b16 v[116:117], v24 offset:20480\n"
    "ds_read_b64_tr_b16 v[118:119], v25 offset:20992\n"
    ".Lw_mm:\n"
    "ds_read_b128 v[40:43], v22\n"
    "ds_read_b128 v[44:47], v22 offset:2048\n"
    "ds_read_b128 v[48:51], v23\n"
    "ds_read_b128 v[52:55], v23 offset:2048\n"
    "ds_read_b128 v[56:59], v22 offset:4096\n"
    "ds_read_b128 v[60:63], v22 offset:6144\n"
    "ds_read_b128 v[64:67], v23 offset:4096\n"
    "ds_read_b128 v[68:71], v23 offset:6144\n"
    "ds_read_b128 v[72:75], v22 offset:8192\n"
    "ds_read_b128 v[76:79], v22 offset:10240\n"
    "ds_read_b128 v[80:83], v23 offset:8192\n"
    "ds_read_b128 v[84:87], v23 offset:10240\n"
    "s_and_b32 s47, s39, 1\n"
    "s_xor_b32 s40, s38, s47\n"
    "s_cmp_eq_u32 s36, 1\n"
    "s_cselect_b32 s40, s38, s40\n"
    "s_lshl_b32 s41, s36, 1\n"
    "s_add_u32 s41, s41, s47\n"
    "s_mul_i32 s41, s41, 6\n"
    "s_and_b32 s42, s39, 6\n"
    "s_add_u32 s41, s41, s42\n"
    "s_add_u32 s41, s41, s13\n"
    "s_mul_i32 s41, s41, 6\n"
    "s_add_u32 s41, s41, s40\n"
    "s_lshl_b32 s41, s41, 10\n"
    "s_add_u32 s41, s41, 24576\n"
    "v_mul_u32_u24 v26, 640, v1\n"
    "s_lshl_b32 s42, s18, 3\n"
    "s_add_u32 s42, s42, 3072\n"
    "s_waitcnt lgkmcnt(11)\n"
    "v_mfma_f32_16x16x32_bf16 v[28:31], v[40:43], v[96:99], 0\n"
    "s_waitcnt lgkmcnt(10)\n"
    "v_mfma_f32_16x16x32_bf16 v[32:35], v[44:47], v[96:99], 0\n"
    "s_waitcnt lgkmcnt(9)\n"
    "v_mfma_f32_16x16x32_bf16 v[28:31], v[48:51], v[100:103], v[28:31]\n"
    "s_waitcnt lgkmcnt(8)\n"
    "v_mfma_f32_16x16x32_bf16 v[32:35], v[52:55], v[100:103], v[32:35]\n"
    "s_waitcnt lgkmcnt(7)\n"
    "v_mfma_f32_16x16x32_bf16 v[28:31], v[56:59], v[104:107], v[28:31]\n"
    "s_waitcnt lgkmcnt(6)\n"
    "v_mfma_f32_16x16x32_bf16 v[32:35], v[60:63], v[104:107], v[32:35]\n"
    "s_waitcnt lgkmcnt(5)\n"
    "v_mfma_f32_16x16x32_bf16 v[28:31], v[64:67], v[108:111], v[28:31]\n"
    "s_waitcnt lgkmcnt(4)\n"
    "v_mfma_f32_16x16x32_bf16 v[32:35], v[68:71], v[108:111], v[32:35]\n"
    "s_waitcnt lgkmcnt(3)\n"
    "v_mfma_f32_16x16x32_bf16 v[28:31], v[72:75], v[112:115], v[28:31]\n"
    "s_waitcnt lgkmcnt(2)\n"
    "v_mfma_f32_16x16x32_bf16 v[32:35], v[76:79], v[112:115], v[32:35]\n"
    "s_waitcnt lgkmcnt(0)\n"
    "v_mfma_f32_16x16x32_bf16 v[28:31], v[80:83], v[116:119], v[28:31]\n"
    "v_mfma_f32_16x16x32_bf16 v[32:35], v[84:87], v[116:119], v[32:35]\n"
    "s_nop 6\n"
    "v_cvt_pk_bf16_f32 v120, v28, v29\n"
    "v_cvt_pk_bf16_f32 v121, v30, v31\n"
    "v_cvt_pk_bf16_f32 v122, v32, v33\n"
    "v_cvt_pk_bf16_f32 v123, v34, v35\n"
    "buffer_store_dwordx4 v[120:123], v17, s[84:87], s41 offen sc1\n"
    "s_waitcnt vmcnt(0)\n"
    "s_barrier\n"
    "s_cmp_lg_u32 s12, 0\n"
    "s_cbranch_scc1 .Lw_seen\n"
    "s_mov_b32 exec_hi, 0\n"
    "buffer_store_dwordx2 v[20:21], v26, s[84:87], s42 offen sc1\n"
    "s_mov_b64 exec, -1\n"
    ".Lw_poll:\n"
    "s_cmp_lg_u32 s12, 0\n"
    "s_cbranch_scc1 .Lw_seen\n"
    "s_and_b32 s47, s2, 31\n"
    "s_mul_i32 s47, s47, 640\n"
    "s_add_u32 s47, s47, 3072\n"
    "s_mov_b32 s46, 0\n"
    "s_mov_b32 exec_lo, -1\n"
    "s_mov_b32 exec_hi, 15\n"
    ".Lw_spin:\n"
    "buffer_load_dwordx4 v[24:27], v17, s[84:87], s47 offen sc1\n"
    "s_waitcnt vmcnt(0)\n"
    "v_cmp_eq_u64 s[40:41], v[24:25], v[20:21]\n"
    "v_cmp_eq_u64 s[42:43], v[26:27], v[20:21]\n"
    "s_add_u32 s46, s46, 1\n"
    "s_and_b64 s[40:41], s[40:41], s[42:43]\n"
    "s_cmp_eq_u64 s[40:41], exec\n"
    "s_cbranch_scc1 .Lw_spun\n"
    "s_sleep 2\n"
    "s_cmp_lt_u32 s46, 0x40000\n"
    "s_cbranch_scc1 .Lw_spin\n"
    ".Lw_spun:\n"
    "s_mov_b64 exec, 1\n"
    "s_lshl_b32 s47, s2, 3\n"
    "s_add_u32 s47, s47, 1024\n"
    "buffer_store_dwordx2 v[20:21], v19, s[84:87], s47 offen sc1\n"
    "s_mov_b64 exec, -1\n"
    ".Lw_seen:\n"
    ".Lw_go:\n"
    "s_mul_i32 s94, s12, 36864\n"
    "s_add_u32 s94, s94, 24576\n"
    "v_mov_b32 v221, v17\n"
    "s_mul_i32 s80, s2, 576\n"
    "s_and_b32 s80, s80, 0xfffffe00\n"
    "s_add_u32 s80, s80, 24576\n"
    "v_lshlrev_b32 v200, 5, v0\n"
    "s_waitcnt vmcnt(12)\n"
    "s_barrier\n"
    "buffer_load_dword v222, v200, s[84:87], s80 offen sc1\n"
    "buffer_load_dwordx4 a[0:3], v221, s[84:87], s94 offen offset:0 sc1\n"
    "ds_read_b128 a[192:195], v254\n"
    "ds_read_b128 a[196:199], v255\n"
    "buffer_load_dwordx4 a[4:7], v221, s[84:87], s94 offen offset:1024 sc1\n"
    "ds_read_b128 a[200:203], v254 offset:128\n"
    "ds_read_b128 a[204:207], v255 offset:128\n"
    "buffer_load_dwordx4 a[8:11], v221, s[84:87], s94 offen offset:2048 sc1\n"
    "ds_read_b128 a[208:211], v254 offset:256\n"
    "ds_read_b128 a[212:215], v255 offset:256\n"
    "buffer_load_dwordx4 a[12:15], v221, s[84:87], s94 offen offset:3072 sc1\n"
    "ds_read_b128 a[144:147], v235\n"
    "ds_read_b128 a[148:151], v236\n"
    "s_add_u32 s94, s94, 4096\n"
    "buffer_load_dwordx4 a[16:19], v221, s[84:87], s94 offen offset:0 sc1\n"
    "ds_read_b128 a[152:155], v235 offset:128\n"
    "ds_read_b128 a[156:159], v236 offset:128\n"
    "buffer_load_dwordx4 a[20:23], v221, s[84:87], s94 offen offset:1024 sc1\n"
    "ds_read_b128 a[160:163], v235 offset:256\n"
    "ds_read_b128 a[164:167], v236 offset:256\n"
    "buffer_load_dwordx4 a[24:27], v221, s[84:87], s94 offen offset:2048 sc1\n"
    "ds_read_b128 a[216:219], v254 offset:6144\n"
    "ds_read_b128 a[220:223], v255 offset:6144\n"
    "buffer_load_dwordx4 a[28:31], v221, s[84:87], s94 offen offset:3072 sc1\n"
    "ds_read_b128 a[224:227], v254 offset:6272\n"
    "ds_read_b128 a[228:231], v255 offset:6272\n"
    "s_add_u32 s94, s94, 4096\n"
    "buffer_load_dwordx4 a[32:35], v221, s[84:87], s94 offen offset:0 sc1\n"
    "ds_read_b128 a[232:235], v254 offset:6400\n"
    "ds_read_b128 a[236:239], v255 offset:6400\n"
    "buffer_load_dwordx4 a[36:39], v221, s[84:87], s94 offen offset:1024 sc1\n"
    "ds_read_b128 a[168:171], v235 offset:6144\n"
    "ds_read_b128 a[172:175], v236 offset:6144\n"
    "buffer_load_dwordx4 a[40:43], v221, s[84:87], s94 offen offset:2048 sc1\n"
    "ds_read_b128 a[176:179], v235 offset:6272\n"
    "ds_read_b128 a[180:183], v236 offset:6272\n"
    "buffer_load_dwordx4 a[44:47], v221, s[84:87], s94 offen offset:3072 sc1\n"
    "ds_read_b128 a[184:187], v235 offset:6400\n"
    "ds_read_b128 a[188:191], v236 offset:6400\n"
    "s_cbranch_vccnz .Lw_entryB\n"
    ".Lw_entryA:\n"
    "v_mov_b32 v220, v243\n"
    "s_mov_b32 s69, 56623104\n"
    "s_mov_b32 s60, 0\n"
    "s_mov_b32 s61, 0\n"
    "s_mov_b32 s62, 98304\n"
    "s_mov_b32 s76, 12288\n"
    "s_mov_b32 s77, 8192\n"
    "s_branch .Lw_T0\n"
    ".Lw_S:\n"
    "ds_read_b128 v[72:75], v240\n"
    "ds_read_b128 v[76:79], v240 offset:1024\n"
    "v_mfma_f32_16x16x32_bf16 v[16:19], a[144:147], v[48:51], 0\n"
    "ds_read_b128 v[80:83], v240 offset:2048\n"
    "ds_read_b128 v[84:87], v240 offset:3072\n"
    "v_mfma_f32_16x16x32_bf16 v[16:19], a[152:155], v[52:55], v[16:19]\n"
    "ds_read_b128 v[88:91], v240 offset:4096\n"
    "ds_read_b128 v[92:95], v240 offset:5120\n"
    "v_mfma_f32_16x16x32_bf16 v[16:19], a[160:163], v[56:59], v[16:19]\n"
    "s_mov_b32 s63, s61\n"
    "s_add_u32 s61, s61, 24576\n"
    "s_cmp_eq_u32 s61, 122880\n"
    "s_cselect_b32 s61, 0, s61\n"
    "v_mfma_f32_16x16x32_bf16 v[20:23], a[168:171], v[48:51], 0\n"
    "s_sub_u32 s68, s61, s63\n"
    "v_mfma_f32_16x16x32_bf16 v[20:23], a[176:179], v[52:55], v[20:23]\n"
    "v_add_u32 v235, s68, v235\n"
    "v_mfma_f32_16x16x32_bf16 v[20:23], a[184:187], v[56:59], v[20:23]\n"
    "v_add_u32 v236, s68, v236\n"
    "v_mfma_f32_16x16x32_bf16 v[24:27], a[192:195], v[48:51], 0\n"
    "v_add_u32 v237, s68, v237\n"
    "v_mfma_f32_16x16x32_bf16 v[24:27], a[200:203], v[52:55], v[24:27]\n"
    "v_add_u32 v238, s68, v238\n"
    "v_mfma_f32_16x16x32_bf16 v[24:27], a[208:211], v[56:59], v[24:27]\n"
    "v_add_u32 v240, s76, v240\n"
    "v_mfma_f32_16x16x32_bf16 v[28:31], a[216:219], v[48:51], 0\n"
    "v_add_u32 v239, s76, v239\n"
    "v_mfma_f32_16x16x32_bf16 v[28:31], a[224:227], v[52:55], v[28:31]\n"
    "s_sub_u32 s76, 0, s76\n"
    "v_mfma_f32_16x16x32_bf16 v[28:31], a[232:235], v[56:59], v[28:31]\n"
    "v_cvt_pk_bf16_f32 v60, v168, v169\n"
    "s_waitcnt lgkmcnt(0)\n"
    "v_mfma_f32_16x16x32_bf16 v[16:19], a[148:151], v[72:75], v[16:19]\n"
    "v_cvt_pk_bf16_f32 v61, v170, v171\n"
    "v_mfma_f32_16x16x32_bf16 v[16:19], a[156:159], v[76:79], v[16:19]\n"
    "v_cvt_pk_bf16_f32 v62, v172, v173\n"
    "v_mfma_f32_16x16x32_bf16 v[16:19], a[164:167], v[80:83], v[16:19]\n"
    "v_cvt_pk_bf16_f32 v63, v174, v175\n"
    "v_mfma_f32_16x16x32_bf16 v[20:23], a[172:175], v[72:75], v[20:23]\n"
    "v_cvt_pk_bf16_f32 v64, v176, v177\n"
    "v_mfma_f32_16x16x32_bf16 v[20:23], a[180:183], v[76:79], v[20:23]\n"
    "v_cvt_pk_bf16_f32 v65, v178, v179\n"
    "v_mfma_f32_16x16x32_bf16 v[20:23], a[188:191], v[80:83], v[20:23]\n"
    "v_cvt_pk_bf16_f32 v66, v180, v181\n"
    "v_mfma_f32_16x16x32_bf16 v[24:27], a[196:199], v[72:75], v[24:27]\n"
    "v_cvt_pk_bf16_f32 v67, v182, v183\n"
    "v_mfma_f32_16x16x32_bf16 v[24:27], a[204:207], v[76:79], v[24:27]\n"
    "v_cvt_pk_bf16_f32 v68, v184, v185\n"
    "v_mfma_f32_16x16x32_bf16 v[24:27], a[212:215], v[80:83], v[24:27]\n"
    "v_cvt_pk_bf16_f32 v69, v186, v187\n"
    "v_mfma_f32_16x16x32_bf16 v[28:31], a[220:223], v[72:75], v[28:31]\n"
    "v_cvt_pk_bf16_f32 v70, v188, v189\n"
    "v_mfma_f32_16x16x32_bf16 v[28:31], a[228:231], v[76:79], v[28:31]\n"
    "v_cvt_pk_bf16_f32 v71, v190, v191\n"
    "v_mfma_f32_16x16x32_bf16 v[28:31], a[236:239], v[80:83], v[28:31]\n"
    "v_add_u32 v220, s63, v243\n"
    "v_mfma_f32_16x16x32_bf16 v[40:43], a[192:195], v[60:63], 0\n"
    "s_add_u32 s69, s54, 884736\n"
    "s_cmp_ge_u32 s60, 2\n"
    "s_cselect_b32 s69, s69, 56623104\n"
    "v_mfma_f32_16x16x32_bf16 v[40:43], a[200:203], v[64:67], v[40:43]\n"
    "s_add_u32 s74, s54, 5947392\n"
    "s_add_u32 s75, s55, 16\n"
    "ds_read_b128 a[192:195], v237\n"
    "v_mfma_f32_16x16x32_bf16 v[40:43], a[208:211], v[68:71], v[40:43]\n"
    "s_cmp_ge_u32 s75, 48\n"
    "s_cselect_b32 s66, 1032192, 0\n"
    "s_cselect_b32 s67, 48, 0\n"
    "v_max3_f32 v120, v16, v17, v18\n"
    "v_mfma_f32_16x16x32_bf16 v[40:43], a[196:199], v[84:87], v[40:43]\n"
    "s_add_u32 s74, s74, s66\n"
    "s_sub_u32 s75, s75, s67\n"
    "v_max3_f32 v121, v19, v20, v21\n"
    "v_mfma_f32_16x16x32_bf16 v[40:43], a[204:207], v[88:91], v[40:43]\n"
    "s_cmp_ge_u32 s60, 2\n"
    "s_cselect_b32 s54, s74, s54\n"
    "s_cselect_b32 s55, s75, s55\n"
    "v_max3_f32 v122, v22, v23, v24\n"
    "v_mfma_f32_16x16x32_bf16 v[40:43], a[212:215], v[92:95], v[40:43]\n"
    "v_max3_f32 v123, v25, v26, v27\n"
    "v_max3_f32 v120, v120, v28, v29\n"
    "v_mfma_f32_16x16x32_bf16 v[44:47], a[216:219], v[60:63], 0\n"
    "v_max3_f32 v121, v121, v30, v31\n"
    "v_max3_f32 v120, v120, v122, v123\n"
    "v_mfma_f32_16x16x32_bf16 v[44:47], a[224:227], v[64:67], v[44:47]\n"
    "v_max_f32 v120, v120, v121\n"
    "ds_read_b128 a[200:203], v237 offset:128\n"
    "v_mfma_f32_16x16x32_bf16 v[44:47], a[232:235], v[68:71], v[44:47]\n"
    "v_mov_b32 v121, v120\n"
    "v_mfma_f32_16x16x32_bf16 v[44:47], a[220:223], v[84:87], v[44:47]\n"
    "ds_read_b128 a[208:211], v237 offset:256\n"
    "ds_read_b128 a[196:199], v238\n"
    "v_mfma_f32_16x16x32_bf16 v[44:47], a[228:231], v[88:91], v[44:47]\n"
    "ds_read_b128 a[204:207], v238 offset:128\n"
    "ds_read_b128 a[212:215], v238 offset:256\n"
    "v_mfma_f32_16x16x32_bf16 v[44:47], a[236:239], v[92:95], v[44:47]\n"
    "s_nop 0\n"
    "v_permlane16_swap_b32 v120, v121\n"
    "v_mfma_f32_16x16x32_bf16 v[32:35], a[144:147], v[60:63], 0\n"
    "v_max_f32 v120, v120, v121\n"
    "ds_read_b128 a[216:219], v237 offset:6144\n"
    "v_mfma_f32_16x16x32_bf16 v[32:35], a[152:155], v[64:67], v[32:35]\n"
    "v_mov_b32 v121, v120\n"
    "v_mfma_f32_16x16x32_bf16 v[32:35], a[160:163], v[68:71], v[32:35]\n"
    "s_nop 0\n"
    "v_permlane32_swap_b32 v120, v121\n"
    "v_mfma_f32_16x16x32_bf16 v[32:35], a[148:151], v[84:87], v[32:35]\n"
    "v_max_f32 v120, v120, v121\n"
    "v_mul_f32 v124, 0xbdd53b94, v120\n"
    "v_mfma_f32_16x16x32_bf16 v[32:35], a[156:159], v[88:91], v[32:35]\n"
    "v_fmamk_f32 v16, v16, 0x3dd53b94, v124\n"
    "v_fmamk_f32 v17, v17, 0x3dd53b94, v124\n"
    "v_mfma_f32_16x16x32_bf16 v[32:35], a[164:167], v[92:95], v[32:35]\n"
    "v_exp_f32 v16, v16\n"
    "v_mfma_f32_16x16x32_bf16 v[36:39], a[168:171], v[60:63], 0\n"
    "v_fmamk_f32 v18, v18, 0x3dd53b94, v124\n"
    "ds_read_b128 a[224:227], v237 offset:6272\n"
    "v_mfma_f32_16x16x32_bf16 v[36:39], a[176:179], v[64:67], v[36:39]\n"
    "ds_read_b128 a[232:235], v237 offset:6400\n"
    "ds_read_b128 a[220:223], v238 offset:6144\n"
    "v_mfma_f32_16x16x32_bf16 v[36:39], a[184:187], v[68:71], v[36:39]\n"
    "ds_read_b128 a[228:231], v238 offset:6272\n"
    "ds_read_b128 a[236:239], v238 offset:6400\n"
    "v_mfma_f32_16x16x32_bf16 v[36:39], a[172:175], v[84:87], v[36:39]\n"
    "ds_read_b128 a[144:147], v235\n"
    "ds_read_b128 a[152:155], v235 offset:128\n"
    "v_mfma_f32_16x16x32_bf16 v[36:39], a[180:183], v[88:91], v[36:39]\n"
    "ds_read_b128 a[160:163], v235 offset:256\n"
    "ds_read_b128 a[148:151], v236\n"
    "ds_read_b128 a[156:159], v236 offset:128\n"
    "ds_read_b128 a[164:167], v236 offset:256\n"
    "v_mfma_f32_16x16x32_bf16 v[36:39], a[188:191], v[92:95], v[36:39]\n"
    "v_exp_f32 v17, v17\n"
    "s_cmp_eq_u32 s60, 9\n"
    "s_cbranch_scc1 .Lw_EPIA\n"
    ".Lw_T:\n"
    "s_waitcnt lgkmcnt(11)\n"
    "v_mfma_f32_16x16x32_bf16 v[160:163], a[0:3], a[192:195], 0\n"
    "v_fmamk_f32 v19, v19, 0x3dd53b94, v124\n"
    "ds_read_b128 a[168:171], v235 offset:6144\n"
    "v_mfma_f32_16x16x32_bf16 v[160:163], a[8:11], a[200:203], v[160:163]\n"
    "v_exp_f32 v18, v18\n"
    "v_mfma_f32_16x16x32_bf16 v[160:163], a[16:19], a[208:211], v[160:163]\n"
    "v_fmamk_f32 v20, v20, 0x3dd53b94, v124\n"
    "ds_read_b128 a[176:179], v235 offset:6272\n"
    "v_mfma_f32_16x16x32_bf16 v[160:163], a[4:7], a[196:199], v[160:163]\n"
    "v_exp_f32 v19, v19\n"
    "v_mfma_f32_16x16x32_bf16 v[160:163], a[12:15], a[204:207], v[160:163]\n"
    "v_fmamk_f32 v21, v21, 0x3dd53b94, v124\n"
    "v_add_f32 v120, v16, v18\n"
    "v_mfma_f32_16x16x32_bf16 v[160:163], a[20:23], a[212:215], v[160:163]\n"
    "v_exp_f32 v20, v20\n"
    "v_mfma_f32_16x16x32_bf16 v[164:167], a[24:27], a[192:195], 0\n"
    "v_fmamk_f32 v22, v22, 0x3dd53b94, v124\n"
    "v_add_f32 v121, v17, v19\n"
    "v_mfma_f32_16x16x32_bf16 v[164:167], a[32:35], a[200:203], v[164:167]\n"
    "v_exp_f32 v21, v21\n"
    "v_mfma_f32_16x16x32_bf16 v[164:167], a[40:43], a[208:211], v[164:167]\n"
    "v_fmamk_f32 v23, v23, 0x3dd53b94, v124\n"
    "v_add_f32 v120, v120, v20\n"
    "v_mfma_f32_16x16x32_bf16 v[164:167], a[28:31], a[196:199], v[164:167]\n"
    "v_exp_f32 v22, v22\n"
    "v_mfma_f32_16x16x32_bf16 v[164:167], a[36:39], a[204:207], v[164:167]\n"
    "v_fmamk_f32 v24, v24, 0x3dd53b94, v124\n"
    "v_add_f32 v121, v121, v21\n"
    "v_mfma_f32_16x16x32_bf16 v[164:167], a[44:47], a[212:215], v[164:167]\n"
    "v_exp_f32 v23, v23\n"
    "v_mfma_f32_16x16x32_bf16 v[168:171], a[48:51], a[192:195], 0\n"
    "v_fmamk_f32 v25, v25, 0x3dd53b94, v124\n"
    "v_add_f32 v120, v120, v22\n"
    "v_mfma_f32_16x16x32_bf16 v[168:171], a[56:59], a[200:203], v[168:171]\n"
    "v_exp_f32 v24, v24\n"
    "v_mfma_f32_16x16x32_bf16 v[168:171], a[64:67], a[208:211], v[168:171]\n"
    "v_fmamk_f32 v26, v26, 0x3dd53b94, v124\n"
    "v_add_f32 v121, v121, v23\n"
    "v_mfma_f32_16x16x32_bf16 v[168:171], a[52:55], a[196:199], v[168:171]\n"
    "v_exp_f32 v25, v25\n"
    "v_mfma_f32_16x16x32_bf16 v[168:171], a[60:63], a[204:207], v[168:171]\n"
    "v_fmamk_f32 v27, v27, 0x3dd53b94, v124\n"
    "v_add_f32 v120, v120, v24\n"
    "v_mfma_f32_16x16x32_bf16 v[168:171], a[68:71], a[212:215], v[168:171]\n"
    "v_exp_f32 v26, v26\n"
    "v_mfma_f32_16x16x32_bf16 v[172:175], a[72:75], a[192:195], 0\n"
    "v_fmamk_f32 v28, v28, 0x3dd53b94, v124\n"
    "v_add_f32 v121, v121, v25\n"
    "v_mfma_f32_16x16x32_bf16 v[172:175], a[80:83], a[200:203], v[172:175]\n"
    "v_exp_f32 v27, v27\n"
    "v_mfma_f32_16x16x32_bf16 v[172:175], a[88:91], a[208:211], v[172:175]\n"
    "v_fmamk_f32 v29, v29, 0x3dd53b94, v124\n"
    "v_add_f32 v120, v120, v26\n"
    "v_mfma_f32_16x16x32_bf16 v[172:175], a[76:79], a[196:199], v[172:175]\n"
    "v_exp_f32 v28, v28\n"
    "v_mfma_f32_16x16x32_bf16 v[172:175], a[84:87], a[204:207], v[172:175]\n"
    "v_cvt_pk_bf16_f32 v96, v160, v161\n"
    "v_cvt_pk_bf16_f32 v97, v162, v163\n"
    "v_mfma_f32_16x16x32_bf16 v[172:175], a[92:95], a[212:215], v[172:175]\n"
    "v_fmamk_f32 v30, v30, 0x3dd53b94, v124\n"
    "v_add_f32 v121, v121, v27\n"
    "v_mfma_f32_16x16x32_bf16 v[176:179], a[96:99], a[192:195], 0\n"
    "v_exp_f32 v29, v29\n"
    "v_mfma_f32_16x16x32_bf16 v[176:179], a[104:107], a[200:203], v[176:179]\n"
    "v_fmamk_f32 v31, v31, 0x3dd53b94, v124\n"
    "v_add_f32 v120, v120, v28\n"
    "v_mfma_f32_16x16x32_bf16 v[176:179], a[112:115], a[208:211], v[176:179]\n"
    "v_exp_f32 v30, v30\n"
    "v_mfma_f32_16x16x32_bf16 v[176:179], a[100:103], a[196:199], v[176:179]\n"
    "v_add_f32 v121, v121, v29\n"
    "v_max3_f32 v126, v32, v33, v34\n"
    "v_mfma_f32_16x16x32_bf16 v[176:179], a[108:111], a[204:207], v[176:179]\n"
    "v_cvt_pk_bf16_f32 v98, v164, v165\n"
    "v_cvt_pk_bf16_f32 v99, v166, v167\n"
    "v_mfma_f32_16x16x32_bf16 v[176:179], a[116:119], a[212:215], v[176:179]\n"
    "ds_write_b128 v239, v[96:99]\n"
    "v_mfma_f32_16x16x32_bf16 v[180:183], a[120:123], a[192:195], 0\n"
    "v_exp_f32 v31, v31\n"
    "v_mfma_f32_16x16x32_bf16 v[180:183], a[128:131], a[200:203], v[180:183]\n"
    "v_add_f32 v120, v120, v30\n"
    "v_add_f32 v120, v120, v121\n"
    "v_mfma_f32_16x16x32_bf16 v[180:183], a[136:139], a[208:211], v[180:183]\n"
    "v_add_f32 v120, v120, v31\n"
    "v_max3_f32 v127, v35, v36, v37\n"
    "v_mfma_f32_16x16x32_bf16 v[180:183], a[124:127], a[196:199], v[180:183]\n"
    "v_mov_b32 v121, v120\n"
    "v_mfma_f32_16x16x32_bf16 v[180:183], a[132:135], a[204:207], v[180:183]\n"
    "v_cvt_pk_bf16_f32 v100, v168, v169\n"
    "v_cvt_pk_bf16_f32 v101, v170, v171\n"
    "v_mfma_f32_16x16x32_bf16 v[180:183], a[140:143], a[212:215], v[180:183]\n"
    "s_nop 0\n"
    "v_permlane16_swap_b32 v120, v121\n"
    "v_mfma_f32_16x16x32_bf16 v[184:187], a[0:3], a[216:219], 0\n"
    "v_add_f32 v120, v120, v121\n"
    "v_max3_f32 v128, v38, v39, v40\n"
    "s_waitcnt lgkmcnt(1)\n"
    "v_mfma_f32_16x16x32_bf16 v[184:187], a[8:11], a[224:227], v[184:187]\n"
    "v_mov_b32 v121, v120\n"
    "v_mfma_f32_16x16x32_bf16 v[184:187], a[16:19], a[232:235], v[184:187]\n"
    "s_nop 0\n"
    "v_permlane32_swap_b32 v120, v121\n"
    "v_mfma_f32_16x16x32_bf16 v[184:187], a[4:7], a[220:223], v[184:187]\n"
    "v_add_f32 v120, v120, v121\n"
    "v_max3_f32 v129, v41, v42, v43\n"
    "v_mfma_f32_16x16x32_bf16 v[184:187], a[12:15], a[228:231], v[184:187]\n"
    "v_cvt_pk_bf16_f32 v102, v172, v173\n"
    "v_cvt_pk_bf16_f32 v103, v174, v175\n"
    "v_mfma_f32_16x16x32_bf16 v[184:187], a[20:23], a[236:239], v[184:187]\n"
    "ds_write_b128 v239, v[100:103] offset:1024\n"
    "v_mfma_f32_16x16x32_bf16 v[188:191], a[24:27], a[216:219], 0\n"
    "v_rcp_f32 v125, v120\n"
    "v_mfma_f32_16x16x32_bf16 v[188:191], a[32:35], a[224:227], v[188:191]\n"
    "v_mul_f32 v16, v125, v16\n"
    "v_mul_f32 v17, v125, v17\n"
    "v_mfma_f32_16x16x32_bf16 v[188:191], a[40:43], a[232:235], v[188:191]\n"
    "v_mul_f32 v18, v125, v18\n"
    "v_mul_f32 v19, v125, v19\n"
    "v_mfma_f32_16x16x32_bf16 v[188:191], a[28:31], a[220:223], v[188:191]\n"
    "v_cvt_pk_bf16_f32 v104, v16, v17\n"
    "v_cvt_pk_bf16_f32 v105, v18, v19\n"
    "v_mfma_f32_16x16x32_bf16 v[188:191], a[36:39], a[228:231], v[188:191]\n"
    "v_cvt_pk_bf16_f32 v96, v176, v177\n"
    "v_cvt_pk_bf16_f32 v97, v178, v179\n"
    "v_mfma_f32_16x16x32_bf16 v[188:191], a[44:47], a[236:239], v[188:191]\n"
    "v_mul_f32 v20, v125, v20\n"
    "v_mul_f32 v21, v125, v21\n"
    "v_mfma_f32_16x16x32_bf16 v[160:163], a[48:51], a[216:219], 0\n"
    "v_mul_f32 v22, v125, v22\n"
    "v_mul_f32 v23, v125, v23\n"
    "v_mfma_f32_16x16x32_bf16 v[160:163], a[56:59], a[224:227], v[160:163]\n"
    "v_cvt_pk_bf16_f32 v106, v20, v21\n"
    "v_cvt_pk_bf16_f32 v107, v22, v23\n"
    "v_mfma_f32_16x16x32_bf16 v[160:163], a[64:67], a[232:235], v[160:163]\n"
    "ds_write_b128 v241, v[104:107]\n"
    "v_mfma_f32_16x16x32_bf16 v[160:163], a[52:55], a[220:223], v[160:163]\n"
    "v_mul_f32 v24, v125, v24\n"
    "v_mul_f32 v25, v125, v25\n"
    "v_mfma_f32_16x16x32_bf16 v[160:163], a[60:63], a[228:231], v[160:163]\n"
    "v_cvt_pk_bf16_f32 v98, v180, v181\n"
    "v_cvt_pk_bf16_f32 v99, v182, v183\n"
    "v_mfma_f32_16x16x32_bf16 v[160:163], a[68:71], a[236:239], v[160:163]\n"
    "ds_write_b128 v239, v[96:99] offset:2048\n"
    "v_mfma_f32_16x16x32_bf16 v[164:167], a[72:75], a[216:219], 0\n"
    "v_mul_f32 v26, v125, v26\n"
    "v_mul_f32 v27, v125, v27\n"
    "v_mfma_f32_16x16x32_bf16 v[164:167], a[80:83], a[224:227], v[164:167]\n"
    "v_cvt_pk_bf16_f32 v108, v24, v25\n"
    "v_cvt_pk_bf16_f32 v109, v26, v27\n"
    "v_mfma_f32_16x16x32_bf16 v[164:167], a[88:91], a[232:235], v[164:167]\n"
    "v_mul_f32 v28, v125, v28\n"
    "v_mul_f32 v29, v125, v29\n"
    "v_mfma_f32_16x16x32_bf16 v[164:167], a[76:79], a[220:223], v[164:167]\n"
    "v_mul_f32 v30, v125, v30\n"
    "v_mul_f32 v31, v125, v31\n"
    "v_mfma_f32_16x16x32_bf16 v[164:167], a[84:87], a[228:231], v[164:167]\n"
    "v_cvt_pk_bf16_f32 v100, v184, v185\n"
    "v_cvt_pk_bf16_f32 v101, v186, v187\n"
    "v_mfma_f32_16x16x32_bf16 v[164:167], a[92:95], a[236:239], v[164:167]\n"
    "v_cvt_pk_bf16_f32 v110, v28, v29\n"
    "v_cvt_pk_bf16_f32 v111, v30, v31\n"
    "v_mfma_f32_16x16x32_bf16 v[168:171], a[96:99], a[216:219], 0\n"
    "ds_write_b128 v242, v[108:111]\n"
    "v_mfma_f32_16x16x32_bf16 v[168:171], a[104:107], a[224:227], v[168:171]\n"
    "v_max3_f32 v126, v126, v44, v45\n"
    "v_max3_f32 v127, v127, v46, v47\n"
    "v_mfma_f32_16x16x32_bf16 v[168:171], a[112:115], a[232:235], v[168:171]\n"
    "v_max3_f32 v126, v126, v128, v129\n"
    "v_max_f32 v126, v126, v127\n"
    "v_mfma_f32_16x16x32_bf16 v[168:171], a[100:103], a[220:223], v[168:171]\n"
    "v_mov_b32 v127, v126\n"
    "v_mfma_f32_16x16x32_bf16 v[168:171], a[108:111], a[228:231], v[168:171]\n"
    "v_cvt_pk_bf16_f32 v102, v188, v189\n"
    "v_cvt_pk_bf16_f32 v103, v190, v191\n"
    "v_mfma_f32_16x16x32_bf16 v[168:171], a[116:119], a[236:239], v[168:171]\n"
    "ds_write_b128 v239, v[100:103] offset:3072\n"
    "v_mfma_f32_16x16x32_bf16 v[172:175], a[120:123], a[216:219], 0\n"
    "s_nop 0\n"
    "v_permlane16_swap_b32 v126, v127\n"
    "v_mfma_f32_16x16x32_bf16 v[172:175], a[128:131], a[224:227], v[172:175]\n"
    "v_max_f32 v126, v126, v127\n"
    "v_cvt_pk_bf16_f32 v96, v160, v161\n"
    "v_mfma_f32_16x16x32_bf16 v[172:175], a[136:139], a[232:235], v[172:175]\n"
    "v_mov_b32 v127, v126\n"
    "v_mfma_f32_16x16x32_bf16 v[172:175], a[124:127], a[220:223], v[172:175]\n"
    "s_nop 0\n"
    "v_permlane32_swap_b32 v126, v127\n"
    "v_mfma_f32_16x16x32_bf16 v[172:175], a[132:135], a[228:231], v[172:175]\n"
    "v_cvt_pk_bf16_f32 v97, v162, v163\n"
    "v_max_f32 v126, v126, v127\n"
    "v_mfma_f32_16x16x32_bf16 v[172:175], a[140:143], a[236:239], v[172:175]\n"
    "v_mul_f32 v130, 0xbdd53b94, v126\n"
    "v_fmamk_f32 v32, v32, 0x3dd53b94, v130\n"
    "v_mfma_f32_16x16x32_bf16 v[176:179], a[0:3], a[144:147], 0\n"
    "v_fmamk_f32 v33, v33, 0x3dd53b94, v130\n"
    "v_cvt_pk_bf16_f32 v98, v164, v165\n"
    "v_mfma_f32_16x16x32_bf16 v[176:179], a[8:11], a[152:155], v[176:179]\n"
    "v_exp_f32 v32, v32\n"
    "v_mfma_f32_16x16x32_bf16 v[176:179], a[16:19], a[160:163], v[176:179]\n"
    "v_fmamk_f32 v34, v34, 0x3dd53b94, v130\n"
    "v_cvt_pk_bf16_f32 v99, v166, v167\n"
    "v_mfma_f32_16x16x32_bf16 v[176:179], a[4:7], a[148:151], v[176:179]\n"
    "v_exp_f32 v33, v33\n"
    "v_mfma_f32_16x16x32_bf16 v[176:179], a[12:15], a[156:159], v[176:179]\n"
    "ds_write_b128 v239, v[96:99] offset:4096\n"
    "v_mfma_f32_16x16x32_bf16 v[176:179], a[20:23], a[164:167], v[176:179]\n"
    "v_fmamk_f32 v35, v35, 0x3dd53b94, v130\n"
    "v_cvt_pk_bf16_f32 v100, v168, v169\n"
    "v_mfma_f32_16x16x32_bf16 v[180:183], a[24:27], a[144:147], 0\n"
    "v_exp_f32 v34, v34\n"
    "v_mfma_f32_16x16x32_bf16 v[180:183], a[32:35], a[152:155], v[180:183]\n"
    "v_fmamk_f32 v36, v36, 0x3dd53b94, v130\n"
    "v_cvt_pk_bf16_f32 v101, v170, v171\n"
    "v_mfma_f32_16x16x32_bf16 v[180:183], a[40:43], a[160:163], v[180:183]\n"
    "v_exp_f32 v35, v35\n"
    "v_mfma_f32_16x16x32_bf16 v[180:183], a[28:31], a[148:151], v[180:183]\n"
    "v_fmamk_f32 v37, v37, 0x3dd53b94, v130\n"
    "v_add_f32 v126, v32, v34\n"
    "v_mfma_f32_16x16x32_bf16 v[180:183], a[36:39], a[156:159], v[180:183]\n"
    "s_barrier\n"
    "v_cvt_pk_bf16_f32 v102, v172, v173\n"
    "v_mfma_f32_16x16x32_bf16 v[180:183], a[44:47], a[164:167], v[180:183]\n"
    "v_exp_f32 v36, v36\n"
    "v_mfma_f32_16x16x32_bf16 v[184:187], a[48:51], a[144:147], 0\n"
    "v_fmamk_f32 v38, v38, 0x3dd53b94, v130\n"
    "v_add_f32 v127, v33, v35\n"
    "v_mfma_f32_16x16x32_bf16 v[184:187], a[56:59], a[152:155], v[184:187]\n"
    "v_exp_f32 v37, v37\n"
    "v_mfma_f32_16x16x32_bf16 v[184:187], a[64:67], a[160:163], v[184:187]\n"
    "ds_read_b128 a[184:187], v235 offset:6400\n"
    "ds_read_b128 a[172:175], v236 offset:6144\n"
    "v_mfma_f32_16x16x32_bf16 v[184:187], a[52:55], a[148:151], v[184:187]\n"
    "ds_read_b128 a[180:183], v236 offset:6272\n"
    "ds_read_b128 a[188:191], v236 offset:6400\n"
    "v_mfma_f32_16x16x32_bf16 v[184:187], a[60:63], a[156:159], v[184:187]\n"
    "v_cvt_pk_bf16_f32 v103, v174, v175\n"
    "v_fmamk_f32 v39, v39, 0x3dd53b94, v130\n"
    "v_mfma_f32_16x16x32_bf16 v[184:187], a[68:71], a[164:167], v[184:187]\n"
    "ds_write_b128 v239, v[100:103] offset:5120\n"
    "v_mfma_f32_16x16x32_bf16 v[188:191], a[72:75], a[144:147], 0\n"
    "v_add_f32 v126, v126, v36\n"
    "v_cvt_pk_bf16_f32 v48, v176, v177\n"
    "v_mfma_f32_16x16x32_bf16 v[188:191], a[80:83], a[152:155], v[188:191]\n"
    "v_exp_f32 v38, v38\n"
    "v_mfma_f32_16x16x32_bf16 v[188:191], a[88:91], a[160:163], v[188:191]\n"
    "v_fmamk_f32 v40, v40, 0x3dd53b94, v130\n"
    "v_add_f32 v127, v127, v37\n"
    "v_mfma_f32_16x16x32_bf16 v[188:191], a[76:79], a[148:151], v[188:191]\n"
    "v_exp_f32 v39, v39\n"
    "v_mfma_f32_16x16x32_bf16 v[188:191], a[84:87], a[156:159], v[188:191]\n"
    "v_cvt_pk_bf16_f32 v49, v178, v179\n"
    "v_fmamk_f32 v41, v41, 0x3dd53b94, v130\n"
    "v_mfma_f32_16x16x32_bf16 v[188:191], a[92:95], a[164:167], v[188:191]\n"
    "v_add_f32 v126, v126, v38\n"
    "v_cvt_pk_bf16_f32 v50, v180, v181\n"
    "v_mfma_f32_16x16x32_bf16 v[160:163], a[96:99], a[144:147], 0\n"
    "v_exp_f32 v40, v40\n"
    "v_mfma_f32_16x16x32_bf16 v[160:163], a[104:107], a[152:155], v[160:163]\n"
    "v_fmamk_f32 v42, v42, 0x3dd53b94, v130\n"
    "v_add_f32 v127, v127, v39\n"
    "v_mfma_f32_16x16x32_bf16 v[160:163], a[112:115], a[160:163], v[160:163]\n"
    "v_exp_f32 v41, v41\n"
    "v_mfma_f32_16x16x32_bf16 v[160:163], a[100:103], a[148:151], v[160:163]\n"
    "v_fmamk_f32 v43, v43, 0x3dd53b94, v130\n"
    "v_add_f32 v126, v126, v40\n"
    "v_mfma_f32_16x16x32_bf16 v[160:163], a[108:111], a[156:159], v[160:163]\n"
    "v_cvt_pk_bf16_f32 v51, v182, v183\n"
    "v_cvt_pk_bf16_f32 v52, v184, v185\n"
    "v_mfma_f32_16x16x32_bf16 v[160:163], a[116:119], a[164:167], v[160:163]\n"
    "v_exp_f32 v42, v42\n"
    "v_mfma_f32_16x16x32_bf16 v[164:167], a[120:123], a[144:147], 0\n"
    "v_fmamk_f32 v44, v44, 0x3dd53b94, v130\n"
    "v_add_f32 v127, v127, v41\n"
    "v_mfma_f32_16x16x32_bf16 v[164:167], a[128:131], a[152:155], v[164:167]\n"
    "v_exp_f32 v43, v43\n"
    "v_mfma_f32_16x16x32_bf16 v[164:167], a[136:139], a[160:163], v[164:167]\n"
    "v_fmamk_f32 v45, v45, 0x3dd53b94, v130\n"
    "v_add_f32 v126, v126, v42\n"
    "v_mfma_f32_16x16x32_bf16 v[164:167], a[124:127], a[148:151], v[164:167]\n"
    "v_exp_f32 v44, v44\n"
    "v_mfma_f32_16x16x32_bf16 v[164:167], a[132:135], a[156:159], v[164:167]\n"
    "v_cvt_pk_bf16_f32 v53, v186, v187\n"
    "v_fmamk_f32 v46, v46, 0x3dd53b94, v130\n"
    "v_mfma_f32_16x16x32_bf16 v[164:167], a[140:143], a[164:167], v[164:167]\n"
    "v_add_f32 v127, v127, v43\n"
    "v_cvt_pk_bf16_f32 v54, v188, v189\n"
    "v_mfma_f32_16x16x32_bf16 v[168:171], a[0:3], a[168:171], 0\n"
    "v_exp_f32 v45, v45\n"
    "v_mfma_f32_16x16x32_bf16 v[168:171], a[8:11], a[176:179], v[168:171]\n"
    "v_fmamk_f32 v47, v47, 0x3dd53b94, v130\n"
    "v_add_f32 v126, v126, v44\n"
    "s_waitcnt lgkmcnt(0)\n"
    "v_mfma_f32_16x16x32_bf16 v[168:171], a[16:19], a[184:187], v[168:171]\n"
    "v_exp_f32 v46, v46\n"
    "v_mfma_f32_16x16x32_bf16 v[168:171], a[4:7], a[172:175], v[168:171]\n"
    "v_add_f32 v127, v127, v45\n"
    "v_cvt_pk_bf16_f32 v55, v190, v191\n"
    "v_mfma_f32_16x16x32_bf16 v[168:171], a[12:15], a[180:183], v[168:171]\n"
    "v_exp_f32 v47, v47\n"
    "v_mfma_f32_16x16x32_bf16 v[168:171], a[20:23], a[188:191], v[168:171]\n"
    "v_add_f32 v126, v126, v46\n"
    "v_add_f32 v126, v126, v127\n"
    "v_mfma_f32_16x16x32_bf16 v[172:175], a[24:27], a[168:171], 0\n"
    "v_add_f32 v126, v126, v47\n"
    "v_cvt_pk_bf16_f32 v56, v160, v161\n"
    "v_mfma_f32_16x16x32_bf16 v[172:175], a[32:35], a[176:179], v[172:175]\n"
    "v_mov_b32 v127, v126\n"
    "v_mfma_f32_16x16x32_bf16 v[172:175], a[40:43], a[184:187], v[172:175]\n"
    "s_nop 0\n"
    "v_permlane16_swap_b32 v126, v127\n"
    "v_mfma_f32_16x16x32_bf16 v[172:175], a[28:31], a[172:175], v[172:175]\n"
    "v_add_f32 v126, v126, v127\n"
    "v_cvt_pk_bf16_f32 v57, v162, v163\n"
    "v_mfma_f32_16x16x32_bf16 v[172:175], a[36:39], a[180:183], v[172:175]\n"
    "v_mov_b32 v127, v126\n"
    "v_mfma_f32_16x16x32_bf16 v[172:175], a[44:47], a[188:191], v[172:175]\n"
    "s_nop 0\n"
    "v_permlane32_swap_b32 v126, v127\n"
    "v_mfma_f32_16x16x32_bf16 v[176:179], a[48:51], a[168:171], 0\n"
    "v_add_f32 v126, v126, v127\n"
    "v_cvt_pk_bf16_f32 v58, v164, v165\n"
    "v_mfma_f32_16x16x32_bf16 v[176:179], a[56:59], a[176:179], v[176:179]\n"
    "v_rcp_f32 v131, v126\n"
    "v_mfma_f32_16x16x32_bf16 v[176:179], a[64:67], a[184:187], v[176:179]\n"
    "ds_read_b128 v[208:211], v220 offset:18432\n"
    "v_mul_f32 v32, v131, v32\n"
    "v_mfma_f32_16x16x32_bf16 v[176:179], a[52:55], a[172:175], v[176:179]\n"
    "v_mul_f32 v33, v131, v33\n"
    "v_mul_f32 v34, v131, v34\n"
    "v_mfma_f32_16x16x32_bf16 v[176:179], a[60:63], a[180:183], v[176:179]\n"
    "ds_read_b128 v[212:215], v220 offset:19456\n"
    "v_cvt_pk_bf16_f32 v59, v166, v167\n"
    "v_mfma_f32_16x16x32_bf16 v[176:179], a[68:71], a[188:191], v[176:179]\n"
    "v_mul_f32 v35, v131, v35\n"
    "v_cvt_pk_bf16_f32 v112, v32, v33\n"
    "v_mfma_f32_16x16x32_bf16 v[180:183], a[72:75], a[168:171], 0\n"
    "ds_read_b128 v[216:219], v220 offset:20480\n"
    "v_cvt_pk_bf16_f32 v113, v34, v35\n"
    "v_mfma_f32_16x16x32_bf16 v[180:183], a[80:83], a[176:179], v[180:183]\n"
    "v_mul_f32 v36, v131, v36\n"
    "v_mul_f32 v37, v131, v37\n"
    "v_mfma_f32_16x16x32_bf16 v[180:183], a[88:91], a[184:187], v[180:183]\n"
    "v_mul_f32 v38, v131, v38\n"
    "v_mul_f32 v39, v131, v39\n"
    "v_mfma_f32_16x16x32_bf16 v[180:183], a[76:79], a[172:175], v[180:183]\n"
    "v_cvt_pk_bf16_f32 v114, v36, v37\n"
    "v_cvt_pk_bf16_f32 v115, v38, v39\n"
    "v_mfma_f32_16x16x32_bf16 v[180:183], a[84:87], a[180:183], v[180:183]\n"
    "ds_write_b128 v241, v[112:115] offset:2048\n"
    "v_mfma_f32_16x16x32_bf16 v[180:183], a[92:95], a[188:191], v[180:183]\n"
    "v_mul_f32 v40, v131, v40\n"
    "v_mul_f32 v41, v131, v41\n"
    "v_mfma_f32_16x16x32_bf16 v[184:187], a[96:99], a[168:171], 0\n"
    "v_mul_f32 v42, v131, v42\n"
    "v_mul_f32 v43, v131, v43\n"
    "v_mfma_f32_16x16x32_bf16 v[184:187], a[104:107], a[176:179], v[184:187]\n"
    "v_cvt_pk_bf16_f32 v116, v40, v41\n"
    "v_cvt_pk_bf16_f32 v117, v42, v43\n"
    "v_mfma_f32_16x16x32_bf16 v[184:187], a[112:115], a[184:187], v[184:187]\n"
    "s_waitcnt lgkmcnt(2)\n"
    "buffer_store_dwordx4 v[208:211], v228, s[24:27], s69 offen sc1\n"
    "v_mfma_f32_16x16x32_bf16 v[184:187], a[100:103], a[172:175], v[184:187]\n"
    "v_mul_f32 v44, v131, v44\n"
    "v_mul_f32 v45, v131, v45\n"
    "v_mfma_f32_16x16x32_bf16 v[184:187], a[108:111], a[180:183], v[184:187]\n"
    "v_mul_f32 v46, v131, v46\n"
    "v_mul_f32 v47, v131, v47\n"
    "v_mfma_f32_16x16x32_bf16 v[184:187], a[116:119], a[188:191], v[184:187]\n"
    "buffer_store_dwordx4 v[212:215], v229, s[24:27], s69 offen offset:1024 sc1\n"
    "v_mfma_f32_16x16x32_bf16 v[188:191], a[120:123], a[168:171], 0\n"
    "v_cvt_pk_bf16_f32 v118, v44, v45\n"
    "v_cvt_pk_bf16_f32 v119, v46, v47\n"
    "v_mfma_f32_16x16x32_bf16 v[188:191], a[128:131], a[176:179], v[188:191]\n"
    "ds_write_b128 v242, v[116:119] offset:2048\n"
    "v_mfma_f32_16x16x32_bf16 v[188:191], a[136:139], a[184:187], v[188:191]\n"
    "s_waitcnt lgkmcnt(1)\n"
    "buffer_store_dwordx4 v[216:219], v230, s[24:27], s69 offen offset:2048 sc1\n"
    "v_mfma_f32_16x16x32_bf16 v[188:191], a[124:127], a[172:175], v[188:191]\n"
    "v_add_u32 v241, s77, v241\n"
    "v_add_u32 v242, s77, v242\n"
    "v_mfma_f32_16x16x32_bf16 v[188:191], a[132:135], a[180:183], v[188:191]\n"
    "s_sub_u32 s77, 0, s77\n"
    "s_add_u32 s60, s60, 1\n"
    "v_mfma_f32_16x16x32_bf16 v[188:191], a[140:143], a[188:191], v[188:191]\n"
    "s_waitcnt lgkmcnt(0)\n"
    "s_barrier\n"
    "s_branch .Lw_S\n"
    ".Lw_T0:\n"
    "s_waitcnt vmcnt(6)\n"
    "s_waitcnt lgkmcnt(15)\n"
    "v_mfma_f32_16x16x32_bf16 v[0:3], a[0:3], a[192:195], 0\n"
    "s_add_u32 s94, s94, 4096\n"
    "buffer_load_dwordx4 a[48:51], v221, s[84:87], s94 offen offset:0 sc1\n"
    "v_mfma_f32_16x16x32_bf16 v[0:3], a[8:11], a[200:203], v[0:3]\n"
    "v_mfma_f32_16x16x32_bf16 v[0:3], a[16:19], a[208:211], v[0:3]\n"
    "v_mfma_f32_16x16x32_bf16 v[0:3], a[4:7], a[196:199], v[0:3]\n"
    "buffer_load_dwordx4 a[52:55], v221, s[84:87], s94 offen offset:1024 sc1\n"
    "v_mfma_f32_16x16x32_bf16 v[0:3], a[12:15], a[204:207], v[0:3]\n"
    "v_mfma_f32_16x16x32_bf16 v[0:3], a[20:23], a[212:215], v[0:3]\n"
    "s_waitcnt lgkmcnt(11)\n"
    "v_mfma_f32_16x16x32_bf16 v[4:7], a[0:3], a[216:219], 0\n"
    "buffer_load_dwordx4 a[56:59], v221, s[84:87], s94 offen offset:2048 sc1\n"
    "s_waitcnt lgkmcnt(9)\n"
    "v_mfma_f32_16x16x32_bf16 v[4:7], a[8:11], a[224:227], v[4:7]\n"
    "s_waitcnt lgkmcnt(7)\n"
    "v_mfma_f32_16x16x32_bf16 v[4:7], a[16:19], a[232:235], v[4:7]\n"
    "v_mfma_f32_16x16x32_bf16 v[4:7], a[4:7], a[220:223], v[4:7]\n"
    "buffer_load_dwordx4 a[60:63], v221, s[84:87], s94 offen offset:3072 sc1\n"
    "v_cvt_pk_bf16_f32 v96, v0, v1\n"
    "v_mfma_f32_16x16x32_bf16 v[4:7], a[12:15], a[228:231], v[4:7]\n"
    "v_cvt_pk_bf16_f32 v97, v2, v3\n"
    "s_waitcnt lgkmcnt(0)\n"
    "v_mfma_f32_16x16x32_bf16 v[4:7], a[20:23], a[236:239], v[4:7]\n"
    "v_mfma_f32_16x16x32_bf16 v[8:11], a[0:3], a[144:147], 0\n"
    "s_add_u32 s94, s94, 4096\n"
    "buffer_load_dwordx4 a[64:67], v221, s[84:87], s94 offen offset:0 sc1\n"
    "v_mfma_f32_16x16x32_bf16 v[8:11], a[8:11], a[152:155], v[8:11]\n"
    "v_mfma_f32_16x16x32_bf16 v[8:11], a[16:19], a[160:163], v[8:11]\n"
    "v_mfma_f32_16x16x32_bf16 v[8:11], a[4:7], a[148:151], v[8:11]\n"
    "buffer_load_dwordx4 a[68:71], v221, s[84:87], s94 offen offset:1024 sc1\n"
    "s_nop 0\n"
    "v_cvt_pk_bf16_f32 v100, v4, v5\n"
    "v_mfma_f32_16x16x32_bf16 v[8:11], a[12:15], a[156:159], v[8:11]\n"
    "v_cvt_pk_bf16_f32 v101, v6, v7\n"
    "v_mfma_f32_16x16x32_bf16 v[8:11], a[20:23], a[164:167], v[8:11]\n"
    "v_mfma_f32_16x16x32_bf16 v[168:171], a[0:3], a[168:171], 0\n"
    "buffer_load_dwordx4 a[72:75], v221, s[84:87], s94 offen offset:2048 sc1\n"
    "v_mfma_f32_16x16x32_bf16 v[168:171], a[8:11], a[176:179], v[168:171]\n"
    "v_mfma_f32_16x16x32_bf16 v[168:171], a[16:19], a[184:187], v[168:171]\n"
    "v_mfma_f32_16x16x32_bf16 v[168:171], a[4:7], a[172:175], v[168:171]\n"
    "buffer_load_dwordx4 a[76:79], v221, s[84:87], s94 offen offset:3072 sc1\n"
    "s_nop 1\n"
    "v_cvt_pk_bf16_f32 v48, v8, v9\n"
    "v_mfma_f32_16x16x32_bf16 v[168:171], a[12:15], a[180:183], v[168:171]\n"
    "v_cvt_pk_bf16_f32 v49, v10, v11\n"
    "v_mfma_f32_16x16x32_bf16 v[168:171], a[20:23], a[188:191], v[168:171]\n"
    "s_waitcnt vmcnt(8)\n"
    "v_mfma_f32_16x16x32_bf16 v[12:15], a[24:27], a[192:195], 0\n"
    "s_add_u32 s94, s94, 4096\n"
    "buffer_load_dwordx4 a[80:83], v221, s[84:87], s94 offen offset:0 sc1\n"
    "v_mfma_f32_16x16x32_bf16 v[12:15], a[32:35], a[200:203], v[12:15]\n"
    "v_mfma_f32_16x16x32_bf16 v[12:15], a[40:43], a[208:211], v[12:15]\n"
    "v_mfma_f32_16x16x32_bf16 v[12:15], a[28:31], a[196:199], v[12:15]\n"
    "buffer_load_dwordx4 a[84:87], v221, s[84:87], s94 offen offset:1024 sc1\n"
    "v_mfma_f32_16x16x32_bf16 v[12:15], a[36:39], a[204:207], v[12:15]\n"
    "v_mfma_f32_16x16x32_bf16 v[12:15], a[44:47], a[212:215], v[12:15]\n"
    "v_mfma_f32_16x16x32_bf16 v[0:3], a[24:27], a[216:219], 0\n"
    "buffer_load_dwordx4 a[88:91], v221, s[84:87], s94 offen offset:2048 sc1\n"
    "v_mfma_f32_16x16x32_bf16 v[0:3], a[32:35], a[224:227], v[0:3]\n"
    "v_mfma_f32_16x16x32_bf16 v[0:3], a[40:43], a[232:235], v[0:3]\n"
    "v_mfma_f32_16x16x32_bf16 v[0:3], a[28:31], a[220:223], v[0:3]\n"
    "buffer_load_dwordx4 a[92:95], v221, s[84:87], s94 offen offset:3072 sc1\n"
    "s_nop 1\n"
    "v_cvt_pk_bf16_f32 v98, v12, v13\n"
    "v_mfma_f32_16x16x32_bf16 v[0:3], a[36:39], a[228:231], v[0:3]\n"
    "v_cvt_pk_bf16_f32 v99, v14, v15\n"
    "v_mfma_f32_16x16x32_bf16 v[0:3], a[44:47], a[236:239], v[0:3]\n"
    "ds_write_b128 v239, v[96:99]\n"
    "v_mfma_f32_16x16x32_bf16 v[4:7], a[24:27], a[144:147], 0\n"
    "s_add_u32 s94, s94, 4096\n"
    "buffer_load_dwordx4 a[96:99], v221, s[84:87], s94 offen offset:0 sc1\n"
    "v_mfma_f32_16x16x32_bf16 v[4:7], a[32:35], a[152:155], v[4:7]\n"
    "v_mfma_f32_16x16x32_bf16 v[4:7], a[40:43], a[160:163], v[4:7]\n"
    "v_mfma_f32_16x16x32_bf16 v[4:7], a[28:31], a[148:151], v[4:7]\n"
    "buffer_load_dwordx4 a[100:103], v221, s[84:87], s94 offen offset:1024 sc1\n"
    "v_cvt_pk_bf16_f32 v102, v0, v1\n"
    "v_mfma_f32_16x16x32_bf16 v[4:7], a[36:39], a[156:159], v[4:7]\n"
    "v_cvt_pk_bf16_f32 v103, v2, v3\n"
    "v_mfma_f32_16x16x32_bf16 v[4:7], a[44:47], a[164:167], v[4:7]\n"
    "ds_write_b128 v239, v[100:103] offset:3072\n"
    "v_mfma_f32_16x16x32_bf16 v[172:175], a[24:27], a[168:171], 0\n"
    "buffer_load_dwordx4 a[104:107], v221, s[84:87], s94 offen offset:2048 sc1\n"
    "v_mfma_f32_16x16x32_bf16 v[172:175], a[32:35], a[176:179], v[172:175]\n"
    "v_mfma_f32_16x16x32_bf16 v[172:175], a[40:43], a[184:187], v[172:175]\n"
    "v_mfma_f32_16x16x32_bf16 v[172:175], a[28:31], a[172:175], v[172:175]\n"
    "buffer_load_dwordx4 a[108:111], v221, s[84:87], s94 offen offset:3072 sc1\n"
    "s_nop 0\n"
    "v_cvt_pk_bf16_f32 v50, v4, v5\n"
    "v_mfma_f32_16x16x32_bf16 v[172:175], a[36:39], a[180:183], v[172:175]\n"
    "v_cvt_pk_bf16_f32 v51, v6, v7\n"
    "v_mfma_f32_16x16x32_bf16 v[172:175], a[44:47], a[188:191], v[172:175]\n"
    "s_waitcnt vmcnt(10)\n"
    "v_mfma_f32_16x16x32_bf16 v[8:11], a[48:51], a[192:195], 0\n"
    "s_add_u32 s94, s94, 4096\n"
    "buffer_load_dwordx4 a[112:115], v221, s[84:87], s94 offen offset:0 sc1\n"
    "v_mfma_f32_16x16x32_bf16 v[8:11], a[56:59], a[200:203], v[8:11]\n"
    "v_mfma_f32_16x16x32_bf16 v[8:11], a[64:67], a[208:211], v[8:11]\n"
    "v_mfma_f32_16x16x32_bf16 v[8:11], a[52:55], a[196:199], v[8:11]\n"
    "buffer_load_dwordx4 a[116:119], v221, s[84:87], s94 offen offset:1024 sc1\n"
    "v_mfma_f32_16x16x32_bf16 v[8:11], a[60:63], a[204:207], v[8:11]\n"
    "v_mfma_f32_16x16x32_bf16 v[8:11], a[68:71], a[212:215], v[8:11]\n"
    "v_mfma_f32_16x16x32_bf16 v[12:15], a[48:51], a[216:219], 0\n"
    "buffer_load_dwordx4 a[120:123], v221, s[84:87], s94 offen offset:2048 sc1\n"
    "v_mfma_f32_16x16x32_bf16 v[12:15], a[56:59], a[224:227], v[12:15]\n"
    "v_mfma_f32_16x16x32_bf16 v[12:15], a[64:67], a[232:235], v[12:15]\n"
    "v_mfma_f32_16x16x32_bf16 v[12:15], a[52:55], a[220:223], v[12:15]\n"
    "buffer_load_dwordx4 a[124:127], v221, s[84:87], s94 offen offset:3072 sc1\n"
    "s_nop 1\n"
    "v_cvt_pk_bf16_f32 v96, v8, v9\n"
    "v_mfma_f32_16x16x32_bf16 v[12:15], a[60:63], a[228:231], v[12:15]\n"
    "v_cvt_pk_bf16_f32 v97, v10, v11\n"
    "v_mfma_f32_16x16x32_bf16 v[12:15], a[68:71], a[236:239], v[12:15]\n"
    "v_mfma_f32_16x16x32_bf16 v[0:3], a[48:51], a[144:147], 0\n"
    "s_add_u32 s94, s94, 4096\n"
    "buffer_load_dwordx4 a[128:131], v221, s[84:87], s94 offen offset:0 sc1\n"
    "v_mfma_f32_16x16x32_bf16 v[0:3], a[56:59], a[152:155], v[0:3]\n"
    "v_mfma_f32_16x16x32_bf16 v[0:3], a[64:67], a[160:163], v[0:3]\n"
    "v_mfma_f32_16x16x32_bf16 v[0:3], a[52:55], a[148:151], v[0:3]\n"
    "buffer_load_dwordx4 a[132:135], v221, s[84:87], s94 offen offset:1024 sc1\n"
    "s_nop 0\n"
    "v_cvt_pk_bf16_f32 v100, v12, v13\n"
    "v_mfma_f32_16x16x32_bf16 v[0:3], a[60:63], a[156:159], v[0:3]\n"
    "v_cvt_pk_bf16_f32 v101, v14, v15\n"
    "v_mfma_f32_16x16x32_bf16 v[0:3], a[68:71], a[164:167], v[0:3]\n"
    "v_mfma_f32_16x16x32_bf16 v[176:179], a[48:51], a[168:171], 0\n"
    "buffer_load_dwordx4 a[136:139], v221, s[84:87], s94 offen offset:2048 sc1\n"
    "v_mfma_f32_16x16x32_bf16 v[176:179], a[56:59], a[176:179], v[176:179]\n"
    "v_mfma_f32_16x16x32_bf16 v[176:179], a[64:67], a[184:187], v[176:179]\n"
    "v_mfma_f32_16x16x32_bf16 v[176:179], a[52:55], a[172:175], v[176:179]\n"
    "buffer_load_dwordx4 a[140:143], v221, s[84:87], s94 offen offset:3072 sc1\n"
    "s_nop 1\n"
    "v_cvt_pk_bf16_f32 v52, v0, v1\n"
    "v_mfma_f32_16x16x32_bf16 v[176:179], a[60:63], a[180:183], v[176:179]\n"
    "v_cvt_pk_bf16_f32 v53, v2, v3\n"
    "v_mfma_f32_16x16x32_bf16 v[176:179], a[68:71], a[188:191], v[176:179]\n"
    "s_waitcnt vmcnt(12)\n"
    "v_mfma_f32_16x16x32_bf16 v[4:7], a[72:75], a[192:195], 0\n"
    "v_mfma_f32_16x16x32_bf16 v[4:7], a[80:83], a[200:203], v[4:7]\n"
    "v_mfma_f32_16x16x32_bf16 v[4:7], a[88:91], a[208:211], v[4:7]\n"
    "v_mfma_f32_16x16x32_bf16 v[4:7], a[76:79], a[196:199], v[4:7]\n"
    "v_mfma_f32_16x16x32_bf16 v[4:7], a[84:87], a[204:207], v[4:7]\n"
    "v_mfma_f32_16x16x32_bf16 v[4:7], a[92:95], a[212:215], v[4:7]\n"
    "v_mfma_f32_16x16x32_bf16 v[8:11], a[72:75], a[216:219], 0\n"
    "v_mfma_f32_16x16x32_bf16 v[8:11], a[80:83], a[224:227], v[8:11]\n"
    "v_mfma_f32_16x16x32_bf16 v[8:11], a[88:91], a[232:235], v[8:11]\n"
    "v_mfma_f32_16x16x32_bf16 v[8:11], a[76:79], a[220:223], v[8:11]\n"
    "s_nop 3\n"
    "v_cvt_pk_bf16_f32 v98, v4, v5\n"
    "v_cvt_pk_bf16_f32 v99, v6, v7\n"
    "v_mfma_f32_16x16x32_bf16 v[8:11], a[84:87], a[228:231], v[8:11]\n"
    "s_barrier\n"
    "v_mfma_f32_16x16x32_bf16 v[8:11], a[92:95], a[236:239], v[8:11]\n"
    "ds_write_b128 v239, v[96:99] offset:1024\n"
    "v_mfma_f32_16x16x32_bf16 v[12:15], a[72:75], a[144:147], 0\n"
    "v_mfma_f32_16x16x32_bf16 v[12:15], a[80:83], a[152:155], v[12:15]\n"
    "v_mfma_f32_16x16x32_bf16 v[12:15], a[88:91], a[160:163], v[12:15]\n"
    "v_mfma_f32_16x16x32_bf16 v[12:15], a[76:79], a[148:151], v[12:15]\n"
    "s_nop 2\n"
    "v_cvt_pk_bf16_f32 v102, v8, v9\n"
    "v_cvt_pk_bf16_f32 v103, v10, v11\n"
    "v_mfma_f32_16x16x32_bf16 v[12:15], a[84:87], a[156:159], v[12:15]\n"
    "ds_write_b128 v239, v[100:103] offset:4096\n"
    "v_mfma_f32_16x16x32_bf16 v[12:15], a[92:95], a[164:167], v[12:15]\n"
    "v_mfma_f32_16x16x32_bf16 v[180:183], a[72:75], a[168:171], 0\n"
    "v_mfma_f32_16x16x32_bf16 v[180:183], a[80:83], a[176:179], v[180:183]\n"
    "v_mfma_f32_16x16x32_bf16 v[180:183], a[88:91], a[184:187], v[180:183]\n"
    "v_mfma_f32_16x16x32_bf16 v[180:183], a[76:79], a[172:175], v[180:183]\n"
    "s_nop 3\n"
    "v_cvt_pk_bf16_f32 v54, v12, v13\n"
    "v_cvt_pk_bf16_f32 v55, v14, v15\n"
    "v_mfma_f32_16x16x32_bf16 v[180:183], a[84:87], a[180:183], v[180:183]\n"
    "v_mfma_f32_16x16x32_bf16 v[180:183], a[92:95], a[188:191], v[180:183]\n"
    "s_waitcnt vmcnt(6)\n"
    "v_mfma_f32_16x16x32_bf16 v[0:3], a[96:99], a[192:195], 0\n"
    "v_mfma_f32_16x16x32_bf16 v[0:3], a[104:107], a[200:203], v[0:3]\n"
    "v_mfma_f32_16x16x32_bf16 v[0:3], a[112:115], a[208:211], v[0:3]\n"
    "v_mfma_f32_16x16x32_bf16 v[0:3], a[100:103], a[196:199], v[0:3]\n"
    "v_mfma_f32_16x16x32_bf16 v[0:3], a[108:111], a[204:207], v[0:3]\n"
    "v_add_u32 v241, s77, v241\n"
    "v_add_u32 v242, s77, v242\n"
    "v_mfma_f32_16x16x32_bf16 v[0:3], a[116:119], a[212:215], v[0:3]\n"
    "s_sub_u32 s77, 0, s77\n"
    "s_add_u32 s60, s60, 1\n"
    "v_mfma_f32_16x16x32_bf16 v[4:7], a[96:99], a[216:219], 0\n"
    "v_mfma_f32_16x16x32_bf16 v[4:7], a[104:107], a[224:227], v[4:7]\n"
    "v_mfma_f32_16x16x32_bf16 v[4:7], a[112:115], a[232:235], v[4:7]\n"
    "v_mfma_f32_16x16x32_bf16 v[4:7], a[100:103], a[220:223], v[4:7]\n"
    "s_nop 1\n"
    "v_cvt_pk_bf16_f32 v96, v0, v1\n"
    "v_cvt_pk_bf16_f32 v97, v2, v3\n"
    "v_mfma_f32_16x16x32_bf16 v[4:7], a[108:111], a[228:231], v[4:7]\n"
    "v_mfma_f32_16x16x32_bf16 v[4:7], a[116:119], a[236:239], v[4:7]\n"
    "v_mfma_f32_16x16x32_bf16 v[8:11], a[96:99], a[144:147], 0\n"
    "v_mfma_f32_16x16x32_bf16 v[8:11], a[104:107], a[152:155], v[8:11]\n"
    "v_mfma_f32_16x16x32_bf16 v[8:11], a[112:115], a[160:163], v[8:11]\n"
    "v_mfma_f32_16x16x32_bf16 v[8:11], a[100:103], a[148:151], v[8:11]\n"
    "s_nop 3\n"
    "v_cvt_pk_bf16_f32 v100, v4, v5\n"
    "v_cvt_pk_bf16_f32 v101, v6, v7\n"
    "v_mfma_f32_16x16x32_bf16 v[8:11], a[108:111], a[156:159], v[8:11]\n"
    "v_mfma_f32_16x16x32_bf16 v[8:11], a[116:119], a[164:167], v[8:11]\n"
    "v_mfma_f32_16x16x32_bf16 v[184:187], a[96:99], a[168:171], 0\n"
    "v_mfma_f32_16x16x32_bf16 v[184:187], a[104:107], a[176:179], v[184:187]\n"
    "v_mfma_f32_16x16x32_bf16 v[184:187], a[112:115], a[184:187], v[184:187]\n"
    "v_mfma_f32_16x16x32_bf16 v[184:187], a[100:103], a[172:175], v[184:187]\n"
    "s_nop 3\n"
    "v_cvt_pk_bf16_f32 v56, v8, v9\n"
    "v_cvt_pk_bf16_f32 v57, v10, v11\n"
    "v_mfma_f32_16x16x32_bf16 v[184:187], a[108:111], a[180:183], v[184:187]\n"
    "v_mfma_f32_16x16x32_bf16 v[184:187], a[116:119], a[188:191], v[184:187]\n"
    "s_waitcnt vmcnt(0)\n"
    "v_mfma_f32_16x16x32_bf16 v[12:15], a[120:123], a[192:195], 0\n"
    "v_mfma_f32_16x16x32_bf16 v[12:15], a[128:131], a[200:203], v[12:15]\n"
    "v_mfma_f32_16x16x32_bf16 v[12:15], a[136:139], a[208:211], v[12:15]\n"
    "v_mfma_f32_16x16x32_bf16 v[12:15], a[124:127], a[196:199], v[12:15]\n"
    "v_mfma_f32_16x16x32_bf16 v[12:15], a[132:135], a[204:207], v[12:15]\n"
    "v_mfma_f32_16x16x32_bf16 v[12:15], a[140:143], a[212:215], v[12:15]\n"
    "v_mfma_f32_16x16x32_bf16 v[0:3], a[120:123], a[216:219], 0\n"
    "v_mfma_f32_16x16x32_bf16 v[0:3], a[128:131], a[224:227], v[0:3]\n"
    "v_mfma_f32_16x16x32_bf16 v[0:3], a[136:139], a[232:235], v[0:3]\n"
    "v_mfma_f32_16x16x32_bf16 v[0:3], a[124:127], a[220:223], v[0:3]\n"
    "s_nop 3\n"
    "v_cvt_pk_bf16_f32 v98, v12, v13\n"
    "v_cvt_pk_bf16_f32 v99, v14, v15\n"
    "v_mfma_f32_16x16x32_bf16 v[0:3], a[132:135], a[228:231], v[0:3]\n"
    "ds_write_b128 v239, v[96:99] offset:2048\n"
    "v_mfma_f32_16x16x32_bf16 v[0:3], a[140:143], a[236:239], v[0:3]\n"
    "v_mfma_f32_16x16x32_bf16 v[4:7], a[120:123], a[144:147], 0\n"
    "v_mfma_f32_16x16x32_bf16 v[4:7], a[128:131], a[152:155], v[4:7]\n"
    "v_mfma_f32_16x16x32_bf16 v[4:7], a[136:139], a[160:163], v[4:7]\n"
    "v_mfma_f32_16x16x32_bf16 v[4:7], a[124:127], a[148:151], v[4:7]\n"
    "s_nop 3\n"
    "v_cvt_pk_bf16_f32 v102, v0, v1\n"
    "v_cvt_pk_bf16_f32 v103, v2, v3\n"
    "v_mfma_f32_16x16x32_bf16 v[4:7], a[132:135], a[156:159], v[4:7]\n"
    "ds_write_b128 v239, v[100:103] offset:5120\n"
    "v_mfma_f32_16x16x32_bf16 v[4:7], a[140:143], a[164:167], v[4:7]\n"
    "v_mfma_f32_16x16x32_bf16 v[188:191], a[120:123], a[168:171], 0\n"
    "v_mfma_f32_16x16x32_bf16 v[188:191], a[128:131], a[176:179], v[188:191]\n"
    "v_mfma_f32_16x16x32_bf16 v[188:191], a[136:139], a[184:187], v[188:191]\n"
    "v_mfma_f32_16x16x32_bf16 v[188:191], a[124:127], a[172:175], v[188:191]\n"
    "s_nop 3\n"
    "v_cvt_pk_bf16_f32 v58, v4, v5\n"
    "v_cvt_pk_bf16_f32 v59, v6, v7\n"
    "v_mfma_f32_16x16x32_bf16 v[188:191], a[132:135], a[180:183], v[188:191]\n"
    "v_mfma_f32_16x16x32_bf16 v[188:191], a[140:143], a[188:191], v[188:191]\n"
    "s_waitcnt lgkmcnt(0)\n"
    "s_barrier\n"
    "s_branch .Lw_S\n"
    ".Lw_EPIA:\n"
    "s_cmp_lg_u32 s2, 0\n"
    "s_cbranch_scc1 .Lw_nb_1\n"
    "s_mov_b32 s47, 1024\n"
    "buffer_load_dwordx4 v[4:7], v221, s[84:87], s47 offen sc1\n"
    "buffer_load_dwordx4 v[8:11], v221, s[84:87], s47 offen offset:1024 sc1\n"
    ".Lw_nb_1:\n"
    "v_max3_f32 v126, v32, v33, v34\n"
    "v_fmamk_f32 v19, v19, 0x3dd53b94, v124\n"
    "s_nop 2\n"
    "v_max3_f32 v127, v35, v36, v37\n"
    "v_exp_f32 v18, v18\n"
    "v_max3_f32 v128, v38, v39, v40\n"
    "v_fmamk_f32 v20, v20, 0x3dd53b94, v124\n"
    "v_max3_f32 v129, v41, v42, v43\n"
    "v_exp_f32 v19, v19\n"
    "v_max3_f32 v126, v126, v44, v45\n"
    "v_fmamk_f32 v21, v21, 0x3dd53b94, v124\n"
    "v_max3_f32 v127, v127, v46, v47\n"
    "v_add_f32 v120, v16, v18\n"
    "v_max3_f32 v126, v126, v128, v129\n"
    "v_exp_f32 v20, v20\n"
    "v_max_f32 v126, v126, v127\n"
    "v_fmamk_f32 v22, v22, 0x3dd53b94, v124\n"
    "v_mov_b32 v127, v126\n"
    "v_add_f32 v121, v17, v19\n"
    "s_nop 0\n"
    "v_exp_f32 v21, v21\n"
    "v_permlane16_swap_b32 v126, v127\n"
    "v_fmamk_f32 v23, v23, 0x3dd53b94, v124\n"
    "v_max_f32 v126, v126, v127\n"
    "v_add_f32 v120, v120, v20\n"
    "v_mov_b32 v127, v126\n"
    "v_exp_f32 v22, v22\n"
    "s_nop 0\n"
    "v_fmamk_f32 v24, v24, 0x3dd53b94, v124\n"
    "v_permlane32_swap_b32 v126, v127\n"
    "v_add_f32 v121, v121, v21\n"
    "v_max_f32 v126, v126, v127\n"
    "v_exp_f32 v23, v23\n"
    "v_mul_f32 v130, 0xbdd53b94, v126\n"
    "v_fmamk_f32 v25, v25, 0x3dd53b94, v124\n"
    "v_fmamk_f32 v32, v32, 0x3dd53b94, v130\n"
    "v_add_f32 v120, v120, v22\n"
    "v_fmamk_f32 v33, v33, 0x3dd53b94, v130\n"
    "v_exp_f32 v24, v24\n"
    "v_exp_f32 v32, v32\n"
    "v_fmamk_f32 v26, v26, 0x3dd53b94, v124\n"
    "v_fmamk_f32 v34, v34, 0x3dd53b94, v130\n"
    "v_add_f32 v121, v121, v23\n"
    "v_exp_f32 v33, v33\n"
    "v_exp_f32 v25, v25\n"
    "v_fmamk_f32 v35, v35, 0x3dd53b94, v130\n"
    "v_fmamk_f32 v27, v27, 0x3dd53b94, v124\n"
    "v_exp_f32 v34, v34\n"
    "v_add_f32 v120, v120, v24\n"
    "v_fmamk_f32 v36, v36, 0x3dd53b94, v130\n"
    "v_exp_f32 v26, v26\n"
    "v_exp_f32 v35, v35\n"
    "v_fmamk_f32 v28, v28, 0x3dd53b94, v124\n"
    "v_fmamk_f32 v37, v37, 0x3dd53b94, v130\n"
    "v_add_f32 v121, v121, v25\n"
    "v_add_f32 v126, v32, v34\n"
    "v_exp_f32 v27, v27\n"
    "v_exp_f32 v36, v36\n"
    "v_fmamk_f32 v29, v29, 0x3dd53b94, v124\n"
    "v_fmamk_f32 v38, v38, 0x3dd53b94, v130\n"
    "v_add_f32 v120, v120, v26\n"
    "v_add_f32 v127, v33, v35\n"
    "v_exp_f32 v28, v28\n"
    "v_exp_f32 v37, v37\n"
    "v_fmamk_f32 v30, v30, 0x3dd53b94, v124\n"
    "v_fmamk_f32 v39, v39, 0x3dd53b94, v130\n"
    "v_add_f32 v121, v121, v27\n"
    "v_add_f32 v126, v126, v36\n"
    "v_exp_f32 v29, v29\n"
    "v_exp_f32 v38, v38\n"
    "v_fmamk_f32 v31, v31, 0x3dd53b94, v124\n"
    "v_fmamk_f32 v40, v40, 0x3dd53b94, v130\n"
    "v_add_f32 v120, v120, v28\n"
    "v_add_f32 v127, v127, v37\n"
    "v_exp_f32 v30, v30\n"
    "v_exp_f32 v39, v39\n"
    "v_add_f32 v121, v121, v29\n"
    "v_fmamk_f32 v41, v41, 0x3dd53b94, v130\n"
    "v_exp_f32 v31, v31\n"
    "v_add_f32 v126, v126, v38\n"
    "v_add_f32 v120, v120, v30\n"
    "v_exp_f32 v40, v40\n"
    "v_add_f32 v120, v120, v121\n"
    "v_fmamk_f32 v42, v42, 0x3dd53b94, v130\n"
    "v_add_f32 v120, v120, v31\n"
    "v_add_f32 v127, v127, v39\n"
    "v_mov_b32 v121, v120\n"
    "v_exp_f32 v41, v41\n"
    "s_nop 0\n"
    "v_fmamk_f32 v43, v43, 0x3dd53b94, v130\n"
    "v_permlane16_swap_b32 v120, v121\n"
    "v_add_f32 v126, v126, v40\n"
    "v_add_f32 v120, v120, v121\n"
    "v_exp_f32 v42, v42\n"
    "v_mov_b32 v121, v120\n"
    "v_fmamk_f32 v44, v44, 0x3dd53b94, v130\n"
    "s_nop 0\n"
    "v_add_f32 v127, v127, v41\n"
    "v_permlane32_swap_b32 v120, v121\n"
    "v_exp_f32 v43, v43\n"
    "v_add_f32 v120, v120, v121\n"
    "s_barrier\n"
    "v_fmamk_f32 v45, v45, 0x3dd53b94, v130\n"
    "v_rcp_f32 v125, v120\n"
    "v_add_f32 v126, v126, v42\n"
    "v_mul_f32 v16, v125, v16\n"
    "v_exp_f32 v44, v44\n"
    "v_mul_f32 v17, v125, v17\n"
    "v_fmamk_f32 v46, v46, 0x3dd53b94, v130\n"
    "v_mul_f32 v18, v125, v18\n"
    "v_add_f32 v127, v127, v43\n"
    "v_mul_f32 v19, v125, v19\n"
    "v_exp_f32 v45, v45\n"
    "v_cvt_pk_bf16_f32 v104, v16, v17\n"
    "v_fmamk_f32 v47, v47, 0x3dd53b94, v130\n"
    "v_cvt_pk_bf16_f32 v105, v18, v19\n"
    "v_add_f32 v126, v126, v44\n"
    "v_mul_f32 v20, v125, v20\n"
    "v_exp_f32 v46, v46\n"
    "v_mul_f32 v21, v125, v21\n"
    "v_add_f32 v127, v127, v45\n"
    "v_mul_f32 v22, v125, v22\n"
    "v_exp_f32 v47, v47\n"
    "v_mul_f32 v23, v125, v23\n"
    "v_add_f32 v126, v126, v46\n"
    "v_cvt_pk_bf16_f32 v106, v20, v21\n"
    "v_add_f32 v126, v126, v127\n"
    "v_cvt_pk_bf16_f32 v107, v22, v23\n"
    "v_add_f32 v126, v126, v47\n"
    "ds_write_b128 v241, v[104:107]\n"
    "v_mov_b32 v127, v126\n"
    "v_mul_f32 v24, v125, v24\n"
    "s_nop 0\n"
    "v_mul_f32 v25, v125, v25\n"
    "v_permlane16_swap_b32 v126, v127\n"
    "v_mul_f32 v26, v125, v26\n"
    "v_add_f32 v126, v126, v127\n"
    "v_mul_f32 v27, v125, v27\n"
    "v_mov_b32 v127, v126\n"
    "v_cvt_pk_bf16_f32 v108, v24, v25\n"
    "s_nop 0\n"
    "v_cvt_pk_bf16_f32 v109, v26, v27\n"
    "v_permlane32_swap_b32 v126, v127\n"
    "v_mul_f32 v28, v125, v28\n"
    "v_add_f32 v126, v126, v127\n"
    "v_mul_f32 v29, v125, v29\n"
    "v_rcp_f32 v131, v126\n"
    "v_mul_f32 v30, v125, v30\n"
    "v_mul_f32 v32, v131, v32\n"
    "v_mul_f32 v31, v125, v31\n"
    "v_mul_f32 v33, v131, v33\n"
    "v_cvt_pk_bf16_f32 v110, v28, v29\n"
    "v_mul_f32 v34, v131, v34\n"
    "v_cvt_pk_bf16_f32 v111, v30, v31\n"
    "v_mul_f32 v35, v131, v35\n"
    "ds_write_b128 v242, v[108:111]\n"
    "v_cvt_pk_bf16_f32 v112, v32, v33\n"
    "v_cvt_pk_bf16_f32 v113, v34, v35\n"
    "v_mul_f32 v36, v131, v36\n"
    "v_mul_f32 v37, v131, v37\n"
    "v_mul_f32 v38, v131, v38\n"
    "v_mul_f32 v39, v131, v39\n"
    "v_cvt_pk_bf16_f32 v114, v36, v37\n"
    "v_cvt_pk_bf16_f32 v115, v38, v39\n"
    "ds_write_b128 v241, v[112:115] offset:2048\n"
    "v_mul_f32 v40, v131, v40\n"
    "v_mul_f32 v41, v131, v41\n"
    "v_mul_f32 v42, v131, v42\n"
    "v_mul_f32 v43, v131, v43\n"
    "v_cvt_pk_bf16_f32 v116, v40, v41\n"
    "v_cvt_pk_bf16_f32 v117, v42, v43\n"
    "v_mul_f32 v44, v131, v44\n"
    "v_mul_f32 v45, v131, v45\n"
    "v_mul_f32 v46, v131, v46\n"
    "v_mul_f32 v47, v131, v47\n"
    "v_cvt_pk_bf16_f32 v118, v44, v45\n"
    "v_cvt_pk_bf16_f32 v119, v46, v47\n"
    "ds_write_b128 v242, v[116:119] offset:2048\n"
    "s_waitcnt lgkmcnt(0)\n"
    "s_barrier\n"
    "s_cmp_lg_u32 s2, 0\n"
    "s_cbranch_scc1 .Lw_nb_2\n"
    "v_mov_b32 v2, s16\n"
    "v_mov_b32 v3, s17\n"
    "s_mov_b32 s46, 0\n"
    "s_xor_b32 s45, s17, 0x80000000\n"
    "v_mov_b32 v12, s16\n"
    "v_mov_b32 v13, s45\n"
    "v_mov_b32 v14, s16\n"
    "v_mov_b32 v15, s45\n"
    ".Lw_bspin:\n"
    "s_waitcnt vmcnt(0)\n"
    "v_cmp_eq_u64 s[40:41], v[4:5], v[2:3]\n"
    "v_cmp_eq_u64 s[42:43], v[6:7], v[2:3]\n"
    "s_nop 0\n"
    "s_and_b64 s[40:41], s[40:41], s[42:43]\n"
    "v_cmp_eq_u64 s[42:43], v[8:9], v[2:3]\n"
    "v_cmp_eq_u64 s[44:45], v[10:11], v[2:3]\n"
    "s_add_u32 s46, s46, 1\n"
    "s_and_b64 s[40:41], s[40:41], s[42:43]\n"
    "s_and_b64 s[40:41], s[40:41], s[44:45]\n"
    "s_cmp_eq_u64 s[40:41], -1\n"
    "s_cbranch_scc1 .Lw_bdone\n"
    "s_sleep 8\n"
    "buffer_load_dwordx4 v[4:7], v221, s[84:87], s47 offen sc1\n"
    "buffer_load_dwordx4 v[8:11], v221, s[84:87], s47 offen offset:1024 sc1\n"
    "s_cmp_lt_u32 s46, 0x40000\n"
    "s_cbranch_scc1 .Lw_bspin\n"
    ".Lw_bdone:\n"
    "s_mul_i32 s47, s13, 10240\n"
    "s_add_u32 s47, s47, 3072\n"
    "buffer_store_dwordx4 v[12:15], v221, s[84:87], s47 offen offset:0 sc1\n"
    "buffer_store_dwordx4 v[12:15], v221, s[84:87], s47 offen offset:1024 sc1\n"
    "buffer_store_dwordx4 v[12:15], v221, s[84:87], s47 offen offset:2048 sc1\n"
    "buffer_store_dwordx4 v[12:15], v221, s[84:87], s47 offen offset:3072 sc1\n"
    "s_add_u32 s47, s47, 4096\n"
    "buffer_store_dwordx4 v[12:15], v221, s[84:87], s47 offen offset:0 sc1\n"
    "buffer_store_dwordx4 v[12:15], v221, s[84:87], s47 offen offset:1024 sc1\n"
    "buffer_store_dwordx4 v[12:15], v221, s[84:87], s47 offen offset:2048 sc1\n"
    "buffer_store_dwordx4 v[12:15], v221, s[84:87], s47 offen offset:3072 sc1\n"
    "s_add_u32 s47, s47, 4096\n"
    "buffer_store_dwordx4 v[12:15], v221, s[84:87], s47 offen offset:0 sc1\n"
    "buffer_store_dwordx4 v[12:15], v221, s[84:87], s47 offen offset:1024 sc1\n"
    ".Lw_nb_2:\n"
    "s_waitcnt lgkmcnt(0)\n"
    "s_barrier\n"
    "ds_read_b128 v[132:135], v244\n"
    "ds_read_b128 v[136:139], v244 offset:1024\n"
    "ds_read_b128 v[140:143], v244 offset:2048\n"
    "ds_read_b128 v[144:147], v244 offset:6144\n"
    "ds_read_b128 v[148:151], v244 offset:7168\n"
    "ds_read_b128 v[152:155], v244 offset:8192\n"
    "s_add_u32 s69, s57, 0\n"
    "s_waitcnt lgkmcnt(5)\n"
    "buffer_store_dwordx4 v[132:135], v228, s[24:27], s69 offen sc1\n"
    "s_waitcnt lgkmcnt(4)\n"
    "buffer_store_dwordx4 v[136:139], v229, s[24:27], s69 offen offset:1024 sc1\n"
    "s_waitcnt lgkmcnt(3)\n"
    "buffer_store_dwordx4 v[140:143], v230, s[24:27], s69 offen offset:2048 sc1\n"
    "s_add_u32 s69, s57, 294912\n"
    "s_waitcnt lgkmcnt(2)\n"
    "buffer_store_dwordx4 v[144:147], v228, s[24:27], s69 offen sc1\n"
    "s_waitcnt lgkmcnt(1)\n"
    "buffer_store_dwordx4 v[148:151], v229, s[24:27], s69 offen offset:1024 sc1\n"
    "s_waitcnt lgkmcnt(0)\n"
    "buffer_store_dwordx4 v[152:155], v230, s[24:27], s69 offen offset:2048 sc1\n"
    "s_or_b32 s47, s2, s12\n"
    "s_cmp_lg_u32 s47, 0\n"
    "s_cbranch_scc1 .Lw_nb_3\n"
    "s_mov_b32 s47, 1024\n"
    "buffer_store_dwordx4 v[12:15], v221, s[84:87], s47 offen sc1\n"
    "buffer_store_dwordx4 v[12:15], v221, s[84:87], s47 offen offset:1024 sc1\n"
    "s_mov_b64 exec, 1\n"
    "buffer_store_dwordx2 v[2:3], v221, s[84:87], 0 offen sc1\n"
    "s_mov_b64 exec, -1\n"
    ".Lw_nb_3:\n"
    "s_add_u32 s57, s57, 589824\n"
    "v_add_u32 v244, 12288, v244\n"
    "s_waitcnt lgkmcnt(0)\n"
    "s_barrier\n"
    "ds_read_b128 v[132:135], v244\n"
    "ds_read_b128 v[136:139], v244 offset:1024\n"
    "ds_read_b128 v[140:143], v244 offset:2048\n"
    "s_add_u32 s69, s57, 0\n"
    "s_waitcnt lgkmcnt(2)\n"
    "buffer_store_dwordx4 v[132:135], v228, s[24:27], s69 offen sc1\n"
    "s_waitcnt lgkmcnt(1)\n"
    "buffer_store_dwordx4 v[136:139], v229, s[24:27], s69 offen offset:1024 sc1\n"
    "s_waitcnt lgkmcnt(0)\n"
    "buffer_store_dwordx4 v[140:143], v230, s[24:27], s69 offen offset:2048 sc1\n"
    "s_branch .Lswin_end\n"
    ".Lw_entryB:\n"
    "s_mov_b32 s60, 0\n"
    "s_mov_b32 s61, 0\n"
    "s_mov_b32 s62, 49152\n"
    "s_mov_b32 s63, 0\n"
    "s_mov_b32 s77, 8192\n"
    "s_branch .Lw_B2p\n"
    ".Lw_B2p:\n"
    "s_waitcnt vmcnt(6)\n"
    "s_waitcnt lgkmcnt(15)\n"
    "v_mfma_f32_16x16x32_bf16 v[0:3], a[144:147], a[0:3], 0\n"
    "s_add_u32 s94, s94, 4096\n"
    "buffer_load_dwordx4 a[48:51], v221, s[84:87], s94 offen offset:0 sc1\n"
    "v_mfma_f32_16x16x32_bf16 v[0:3], a[148:151], a[4:7], v[0:3]\n"
    "v_mfma_f32_16x16x32_bf16 v[0:3], a[152:155], a[8:11], v[0:3]\n"
    "s_waitcnt lgkmcnt(14)\n"
    "v_mfma_f32_16x16x32_bf16 v[0:3], a[156:159], a[12:15], v[0:3]\n"
    "buffer_load_dwordx4 a[52:55], v221, s[84:87], s94 offen offset:1024 sc1\n"
    "s_waitcnt lgkmcnt(13)\n"
    "v_mfma_f32_16x16x32_bf16 v[0:3], a[160:163], a[16:19], v[0:3]\n"
    "s_waitcnt lgkmcnt(12)\n"
    "v_mfma_f32_16x16x32_bf16 v[0:3], a[164:167], a[20:23], v[0:3]\n"
    "s_waitcnt vmcnt(2)\n"
    "v_mfma_f32_16x16x32_bf16 v[4:7], a[144:147], a[24:27], 0\n"
    "buffer_load_dwordx4 a[56:59], v221, s[84:87], s94 offen offset:2048 sc1\n"
    "v_mfma_f32_16x16x32_bf16 v[4:7], a[148:151], a[28:31], v[4:7]\n"
    "v_mfma_f32_16x16x32_bf16 v[4:7], a[152:155], a[32:35], v[4:7]\n"
    "v_mfma_f32_16x16x32_bf16 v[4:7], a[156:159], a[36:39], v[4:7]\n"
    "buffer_load_dwordx4 a[60:63], v221, s[84:87], s94 offen offset:3072 sc1\n"
    "v_mfma_f32_16x16x32_bf16 v[4:7], a[160:163], a[40:43], v[4:7]\n"
    "v_mfma_f32_16x16x32_bf16 v[4:7], a[164:167], a[44:47], v[4:7]\n"
    "s_waitcnt lgkmcnt(0)\n"
    "v_mfma_f32_16x16x32_bf16 v[24:27], a[168:171], a[0:3], 0\n"
    "s_add_u32 s94, s94, 4096\n"
    "buffer_load_dwordx4 a[64:67], v221, s[84:87], s94 offen offset:0 sc1\n"
    "v_mfma_f32_16x16x32_bf16 v[24:27], a[172:175], a[4:7], v[24:27]\n"
    "v_mfma_f32_16x16x32_bf16 v[24:27], a[176:179], a[8:11], v[24:27]\n"
    "v_mfma_f32_16x16x32_bf16 v[24:27], a[180:183], a[12:15], v[24:27]\n"
    "buffer_load_dwordx4 a[68:71], v221, s[84:87], s94 offen offset:1024 sc1\n"
    "v_mfma_f32_16x16x32_bf16 v[24:27], a[184:187], a[16:19], v[24:27]\n"
    "v_mfma_f32_16x16x32_bf16 v[24:27], a[188:191], a[20:23], v[24:27]\n"
    "v_mfma_f32_16x16x32_bf16 v[28:31], a[168:171], a[24:27], 0\n"
    "buffer_load_dwordx4 a[72:75], v221, s[84:87], s94 offen offset:2048 sc1\n"
    "v_mfma_f32_16x16x32_bf16 v[28:31], a[172:175], a[28:31], v[28:31]\n"
    "v_mfma_f32_16x16x32_bf16 v[28:31], a[176:179], a[32:35], v[28:31]\n"
    "v_mfma_f32_16x16x32_bf16 v[28:31], a[180:183], a[36:39], v[28:31]\n"
    "buffer_load_dwordx4 a[76:79], v221, s[84:87], s94 offen offset:3072 sc1\n"
    "v_mfma_f32_16x16x32_bf16 v[28:31], a[184:187], a[40:43], v[28:31]\n"
    "v_mfma_f32_16x16x32_bf16 v[28:31], a[188:191], a[44:47], v[28:31]\n"
    "v_mfma_f32_16x16x32_bf16 v[48:51], a[192:195], a[0:3], 0\n"
    "s_add_u32 s94, s94, 4096\n"
    "buffer_load_dwordx4 a[80:83], v221, s[84:87], s94 offen offset:0 sc1\n"
    "v_mfma_f32_16x16x32_bf16 v[48:51], a[196:199], a[4:7], v[48:51]\n"
    "v_mfma_f32_16x16x32_bf16 v[48:51], a[200:203], a[8:11], v[48:51]\n"
    "v_mfma_f32_16x16x32_bf16 v[48:51], a[204:207], a[12:15], v[48:51]\n"
    "buffer_load_dwordx4 a[84:87], v221, s[84:87], s94 offen offset:1024 sc1\n"
    "v_mfma_f32_16x16x32_bf16 v[48:51], a[208:211], a[16:19], v[48:51]\n"
    "v_mfma_f32_16x16x32_bf16 v[48:51], a[212:215], a[20:23], v[48:51]\n"
    "v_mfma_f32_16x16x32_bf16 v[52:55], a[192:195], a[24:27], 0\n"
    "buffer_load_dwordx4 a[88:91], v221, s[84:87], s94 offen offset:2048 sc1\n"
    "v_mfma_f32_16x16x32_bf16 v[52:55], a[196:199], a[28:31], v[52:55]\n"
    "v_mfma_f32_16x16x32_bf16 v[52:55], a[200:203], a[32:35], v[52:55]\n"
    "v_mfma_f32_16x16x32_bf16 v[52:55], a[204:207], a[36:39], v[52:55]\n"
    "buffer_load_dwordx4 a[92:95], v221, s[84:87], s94 offen offset:3072 sc1\n"
    "v_mfma_f32_16x16x32_bf16 v[52:55], a[208:211], a[40:43], v[52:55]\n"
    "v_mfma_f32_16x16x32_bf16 v[52:55], a[212:215], a[44:47], v[52:55]\n"
    "v_mfma_f32_16x16x32_bf16 v[72:75], a[216:219], a[0:3], 0\n"
    "s_add_u32 s94, s94, 4096\n"
    "buffer_load_dwordx4 a[96:99], v221, s[84:87], s94 offen offset:0 sc1\n"
    "v_mfma_f32_16x16x32_bf16 v[72:75], a[220:223], a[4:7], v[72:75]\n"
    "v_mfma_f32_16x16x32_bf16 v[72:75], a[224:227], a[8:11], v[72:75]\n"
    "v_mfma_f32_16x16x32_bf16 v[72:75], a[228:231], a[12:15], v[72:75]\n"
    "buffer_load_dwordx4 a[100:103], v221, s[84:87], s94 offen offset:1024 sc1\n"
    "v_mfma_f32_16x16x32_bf16 v[72:75], a[232:235], a[16:19], v[72:75]\n"
    "v_mfma_f32_16x16x32_bf16 v[72:75], a[236:239], a[20:23], v[72:75]\n"
    "v_mfma_f32_16x16x32_bf16 v[76:79], a[216:219], a[24:27], 0\n"
    "buffer_load_dwordx4 a[104:107], v221, s[84:87], s94 offen offset:2048 sc1\n"
    "v_mfma_f32_16x16x32_bf16 v[76:79], a[220:223], a[28:31], v[76:79]\n"
    "v_mfma_f32_16x16x32_bf16 v[76:79], a[224:227], a[32:35], v[76:79]\n"
    "v_mfma_f32_16x16x32_bf16 v[76:79], a[228:231], a[36:39], v[76:79]\n"
    "buffer_load_dwordx4 a[108:111], v221, s[84:87], s94 offen offset:3072 sc1\n"
    "v_mfma_f32_16x16x32_bf16 v[76:79], a[232:235], a[40:43], v[76:79]\n"
    "v_mfma_f32_16x16x32_bf16 v[76:79], a[236:239], a[44:47], v[76:79]\n"
    "s_waitcnt vmcnt(4)\n"
    "v_mfma_f32_16x16x32_bf16 v[8:11], a[144:147], a[48:51], 0\n"
    "s_add_u32 s94, s94, 4096\n"
    "buffer_load_dwordx4 a[112:115], v221, s[84:87], s94 offen offset:0 sc1\n"
    "v_mfma_f32_16x16x32_bf16 v[8:11], a[148:151], a[52:55], v[8:11]\n"
    "v_mfma_f32_16x16x32_bf16 v[8:11], a[152:155], a[56:59], v[8:11]\n"
    "v_mfma_f32_16x16x32_bf16 v[8:11], a[156:159], a[60:63], v[8:11]\n"
    "buffer_load_dwordx4 a[116:119], v221, s[84:87], s94 offen offset:1024 sc1\n"
    "v_mfma_f32_16x16x32_bf16 v[8:11], a[160:163], a[64:67], v[8:11]\n"
    "v_mfma_f32_16x16x32_bf16 v[8:11], a[164:167], a[68:71], v[8:11]\n"
    "v_mfma_f32_16x16x32_bf16 v[12:15], a[144:147], a[72:75], 0\n"
    "buffer_load_dwordx4 a[120:123], v221, s[84:87], s94 offen offset:2048 sc1\n"
    "v_mfma_f32_16x16x32_bf16 v[12:15], a[148:151], a[76:79], v[12:15]\n"
    "v_mfma_f32_16x16x32_bf16 v[12:15], a[152:155], a[80:83], v[12:15]\n"
    "v_mfma_f32_16x16x32_bf16 v[12:15], a[156:159], a[84:87], v[12:15]\n"
    "buffer_load_dwordx4 a[124:127], v221, s[84:87], s94 offen offset:3072 sc1\n"
    "v_mfma_f32_16x16x32_bf16 v[12:15], a[160:163], a[88:91], v[12:15]\n"
    "v_mfma_f32_16x16x32_bf16 v[12:15], a[164:167], a[92:95], v[12:15]\n"
    "v_mfma_f32_16x16x32_bf16 v[32:35], a[168:171], a[48:51], 0\n"
    "s_add_u32 s94, s94, 4096\n"
    "buffer_load_dwordx4 a[128:131], v221, s[84:87], s94 offen offset:0 sc1\n"
    "v_mfma_f32_16x16x32_bf16 v[32:35], a[172:175], a[52:55], v[32:35]\n"
    "v_mfma_f32_16x16x32_bf16 v[32:35], a[176:179], a[56:59], v[32:35]\n"
    "v_mfma_f32_16x16x32_bf16 v[32:35], a[180:183], a[60:63], v[32:35]\n"
    "buffer_load_dwordx4 a[132:135], v221, s[84:87], s94 offen offset:1024 sc1\n"
    "v_mfma_f32_16x16x32_bf16 v[32:35], a[184:187], a[64:67], v[32:35]\n"
    "v_mfma_f32_16x16x32_bf16 v[32:35], a[188:191], a[68:71], v[32:35]\n"
    "v_mfma_f32_16x16x32_bf16 v[36:39], a[168:171], a[72:75], 0\n"
    "buffer_load_dwordx4 a[136:139], v221, s[84:87], s94 offen offset:2048 sc1\n"
    "v_mfma_f32_16x16x32_bf16 v[36:39], a[172:175], a[76:79], v[36:39]\n"
    "v_mfma_f32_16x16x32_bf16 v[36:39], a[176:179], a[80:83], v[36:39]\n"
    "v_mfma_f32_16x16x32_bf16 v[36:39], a[180:183], a[84:87], v[36:39]\n"
    "buffer_load_dwordx4 a[140:143], v221, s[84:87], s94 offen offset:3072 sc1\n"
    "v_mfma_f32_16x16x32_bf16 v[36:39], a[184:187], a[88:91], v[36:39]\n"
    "v_mfma_f32_16x16x32_bf16 v[36:39], a[188:191], a[92:95], v[36:39]\n"
    "v_mfma_f32_16x16x32_bf16 v[56:59], a[192:195], a[48:51], 0\n"
    "s_add_u32 s70, s62, s56\n"
    "v_mfma_f32_16x16x32_bf16 v[56:59], a[196:199], a[52:55], v[56:59]\n"
    "v_mfma_f32_16x16x32_bf16 v[56:59], a[200:203], a[56:59], v[56:59]\n"
    "s_add_u32 m0, s70, 0\n"
    "s_add_u32 s73, s52, 0\n"
    "v_mfma_f32_16x16x32_bf16 v[56:59], a[204:207], a[60:63], v[56:59]\n"
    "buffer_load_dwordx4 v228, s[20:23], s73 offen nt lds\n"
    "v_mfma_f32_16x16x32_bf16 v[56:59], a[208:211], a[64:67], v[56:59]\n"
    "v_mfma_f32_16x16x32_bf16 v[56:59], a[212:215], a[68:71], v[56:59]\n"
    "v_mfma_f32_16x16x32_bf16 v[60:63], a[192:195], a[72:75], 0\n"
    "buffer_load_dwordx4 v229, s[20:23], s73 offen offset:1024 nt lds\n"
    "v_mfma_f32_16x16x32_bf16 v[60:63], a[196:199], a[76:79], v[60:63]\n"
    "v_mfma_f32_16x16x32_bf16 v[60:63], a[200:203], a[80:83], v[60:63]\n"
    "v_mfma_f32_16x16x32_bf16 v[60:63], a[204:207], a[84:87], v[60:63]\n"
    "buffer_load_dwordx4 v230, s[20:23], s73 offen offset:2048 nt lds\n"
    "v_mfma_f32_16x16x32_bf16 v[60:63], a[208:211], a[88:91], v[60:63]\n"
    "v_mfma_f32_16x16x32_bf16 v[60:63], a[212:215], a[92:95], v[60:63]\n"
    "s_add_u32 m0, s70, 6144\n"
    "s_add_u32 s73, s52, 294912\n"
    "v_mfma_f32_16x16x32_bf16 v[80:83], a[216:219], a[48:51], 0\n"
    "buffer_load_dwordx4 v228, s[20:23], s73 offen nt lds\n"
    "v_mfma_f32_16x16x32_bf16 v[80:83], a[220:223], a[52:55], v[80:83]\n"
    "v_mfma_f32_16x16x32_bf16 v[80:83], a[224:227], a[56:59], v[80:83]\n"
    "s_barrier\n"
    "v_mfma_f32_16x16x32_bf16 v[80:83], a[228:231], a[60:63], v[80:83]\n"
    "buffer_load_dwordx4 v229, s[20:23], s73 offen offset:1024 nt lds\n"
    "v_mfma_f32_16x16x32_bf16 v[80:83], a[232:235], a[64:67], v[80:83]\n"
    "v_mfma_f32_16x16x32_bf16 v[80:83], a[236:239], a[68:71], v[80:83]\n"
    "v_mfma_f32_16x16x32_bf16 v[84:87], a[216:219], a[72:75], 0\n"
    "buffer_load_dwordx4 v230, s[20:23], s73 offen offset:2048 nt lds\n"
    "v_mfma_f32_16x16x32_bf16 v[84:87], a[220:223], a[76:79], v[84:87]\n"
    "v_mfma_f32_16x16x32_bf16 v[84:87], a[224:227], a[80:83], v[84:87]\n"
    "s_add_u32 m0, s70, 12288\n"
    "s_add_u32 s73, s52, 589824\n"
    "v_mfma_f32_16x16x32_bf16 v[84:87], a[228:231], a[84:87], v[84:87]\n"
    "buffer_load_dwordx4 v228, s[20:23], s73 offen nt lds\n"
    "v_mfma_f32_16x16x32_bf16 v[84:87], a[232:235], a[88:91], v[84:87]\n"
    "v_mfma_f32_16x16x32_bf16 v[84:87], a[236:239], a[92:95], v[84:87]\n"
    "s_waitcnt vmcnt(7)\n"
    "v_mfma_f32_16x16x32_bf16 v[16:19], a[144:147], a[96:99], 0\n"
    "buffer_load_dwordx4 v229, s[20:23], s73 offen offset:1024 nt lds\n"
    "v_mfma_f32_16x16x32_bf16 v[16:19], a[148:151], a[100:103], v[16:19]\n"
    "v_mfma_f32_16x16x32_bf16 v[16:19], a[152:155], a[104:107], v[16:19]\n"
    "v_mfma_f32_16x16x32_bf16 v[16:19], a[156:159], a[108:111], v[16:19]\n"
    "buffer_load_dwordx4 v230, s[20:23], s73 offen offset:2048 nt lds\n"
    "v_mfma_f32_16x16x32_bf16 v[16:19], a[160:163], a[112:115], v[16:19]\n"
    "v_mfma_f32_16x16x32_bf16 v[16:19], a[164:167], a[116:119], v[16:19]\n"
    "s_add_u32 m0, s70, 18432\n"
    "s_add_u32 s73, s52, 884736\n"
    "v_mfma_f32_16x16x32_bf16 v[20:23], a[144:147], a[120:123], 0\n"
    "buffer_load_dwordx4 v228, s[20:23], s73 offen nt lds\n"
    "v_mfma_f32_16x16x32_bf16 v[20:23], a[148:151], a[124:127], v[20:23]\n"
    "v_mfma_f32_16x16x32_bf16 v[20:23], a[152:155], a[128:131], v[20:23]\n"
    "v_mfma_f32_16x16x32_bf16 v[20:23], a[156:159], a[132:135], v[20:23]\n"
    "buffer_load_dwordx4 v229, s[20:23], s73 offen offset:1024 nt lds\n"
    "v_mfma_f32_16x16x32_bf16 v[20:23], a[160:163], a[136:139], v[20:23]\n"
    "v_mfma_f32_16x16x32_bf16 v[20:23], a[164:167], a[140:143], v[20:23]\n"
    "v_mfma_f32_16x16x32_bf16 v[40:43], a[168:171], a[96:99], 0\n"
    "buffer_load_dwordx4 v230, s[20:23], s73 offen offset:2048 nt lds\n"
    "v_mfma_f32_16x16x32_bf16 v[40:43], a[172:175], a[100:103], v[40:43]\n"
    "s_add_u32 s52, s52, 5947392\n"
    "s_add_u32 s53, s53, 16\n"
    "v_mfma_f32_16x16x32_bf16 v[40:43], a[176:179], a[104:107], v[40:43]\n"
    "s_cmp_ge_u32 s53, 48\n"
    "s_cselect_b32 s66, 1032192, 0\n"
    "s_cselect_b32 s67, 48, 0\n"
    "v_mfma_f32_16x16x32_bf16 v[40:43], a[180:183], a[108:111], v[40:43]\n"
    "s_add_u32 s52, s52, s66\n"
    "s_sub_u32 s53, s53, s67\n"
    "v_mfma_f32_16x16x32_bf16 v[40:43], a[184:187], a[112:115], v[40:43]\n"
    "s_add_u32 s62, s62, 24576\n"
    "s_cmp_eq_u32 s62, 122880\n"
    "s_cselect_b32 s62, 0, s62\n"
    "v_mfma_f32_16x16x32_bf16 v[40:43], a[188:191], a[116:119], v[40:43]\n"
    "v_mfma_f32_16x16x32_bf16 v[44:47], a[168:171], a[120:123], 0\n"
    "v_mfma_f32_16x16x32_bf16 v[44:47], a[172:175], a[124:127], v[44:47]\n"
    "v_mfma_f32_16x16x32_bf16 v[44:47], a[176:179], a[128:131], v[44:47]\n"
    "v_mfma_f32_16x16x32_bf16 v[44:47], a[180:183], a[132:135], v[44:47]\n"
    "v_mfma_f32_16x16x32_bf16 v[44:47], a[184:187], a[136:139], v[44:47]\n"
    "v_mfma_f32_16x16x32_bf16 v[44:47], a[188:191], a[140:143], v[44:47]\n"
    "s_add_u32 s60, s60, 1\n"
    "s_waitcnt lgkmcnt(0)\n"
    "s_barrier\n"
    ".Lw_B1p:\n"
    "v_mfma_f32_16x16x32_bf16 v[64:67], a[192:195], a[96:99], 0\n"
    "v_cvt_pk_bf16_f32 v96, v0, v1\n"
    "v_cvt_pk_bf16_f32 v97, v2, v3\n"
    "v_mfma_f32_16x16x32_bf16 v[64:67], a[196:199], a[100:103], v[64:67]\n"
    "v_cvt_pk_bf16_f32 v104, v4, v5\n"
    "v_cvt_pk_bf16_f32 v105, v6, v7\n"
    "v_mfma_f32_16x16x32_bf16 v[64:67], a[200:203], a[104:107], v[64:67]\n"
    "v_cvt_pk_bf16_f32 v98, v24, v25\n"
    "v_cvt_pk_bf16_f32 v99, v26, v27\n"
    "v_mfma_f32_16x16x32_bf16 v[64:67], a[204:207], a[108:111], v[64:67]\n"
    "s_mov_b32 s64, s63\n"
    "s_mov_b32 s63, s61\n"
    "v_cvt_pk_bf16_f32 v106, v28, v29\n"
    "v_mfma_f32_16x16x32_bf16 v[64:67], a[208:211], a[112:115], v[64:67]\n"
    "s_add_u32 s61, s61, 24576\n"
    "s_cmp_eq_u32 s61, 122880\n"
    "s_cselect_b32 s61, 0, s61\n"
    "v_mfma_f32_16x16x32_bf16 v[64:67], a[212:215], a[116:119], v[64:67]\n"
    "s_sub_u32 s63, s61, s63\n"
    "v_add_u32 v235, s63, v235\n"
    "v_mfma_f32_16x16x32_bf16 v[68:71], a[192:195], a[120:123], 0\n"
    "v_add_u32 v236, s63, v236\n"
    "v_add_u32 v237, s64, v237\n"
    "v_mfma_f32_16x16x32_bf16 v[68:71], a[196:199], a[124:127], v[68:71]\n"
    "v_add_u32 v239, s64, v239\n"
    "v_add_u32 v238, s77, v238\n"
    "s_sub_u32 s77, 0, s77\n"
    "v_mfma_f32_16x16x32_bf16 v[68:71], a[200:203], a[128:131], v[68:71]\n"
    "ds_read_b128 a[144:147], v235\n"
    "ds_read_b128 a[148:151], v236\n"
    "v_mfma_f32_16x16x32_bf16 v[68:71], a[204:207], a[132:135], v[68:71]\n"
    "ds_read_b128 a[152:155], v235 offset:128\n"
    "ds_read_b128 a[156:159], v236 offset:128\n"
    "v_mfma_f32_16x16x32_bf16 v[68:71], a[208:211], a[136:139], v[68:71]\n"
    "ds_read_b128 a[160:163], v235 offset:256\n"
    "ds_read_b128 a[164:167], v236 offset:256\n"
    "v_mfma_f32_16x16x32_bf16 v[68:71], a[212:215], a[140:143], v[68:71]\n"
    "ds_read_b128 a[168:171], v235 offset:6144\n"
    "ds_read_b128 a[172:175], v236 offset:6144\n"
    "v_mfma_f32_16x16x32_bf16 v[88:91], a[216:219], a[96:99], 0\n"
    "ds_read_b128 a[176:179], v235 offset:6272\n"
    "ds_read_b128 a[180:183], v236 offset:6272\n"
    "v_mfma_f32_16x16x32_bf16 v[88:91], a[220:223], a[100:103], v[88:91]\n"
    "ds_read_b128 a[184:187], v235 offset:6400\n"
    "ds_read_b128 a[188:191], v236 offset:6400\n"
    "v_mfma_f32_16x16x32_bf16 v[88:91], a[224:227], a[104:107], v[88:91]\n"
    "v_cvt_pk_bf16_f32 v107, v30, v31\n"
    "v_mfma_f32_16x16x32_bf16 v[88:91], a[228:231], a[108:111], v[88:91]\n"
    "v_mfma_f32_16x16x32_bf16 v[88:91], a[232:235], a[112:115], v[88:91]\n"
    "v_mfma_f32_16x16x32_bf16 v[88:91], a[236:239], a[116:119], v[88:91]\n"
    "v_mfma_f32_16x16x32_bf16 v[92:95], a[216:219], a[120:123], 0\n"
    "v_mfma_f32_16x16x32_bf16 v[92:95], a[220:223], a[124:127], v[92:95]\n"
    "v_mfma_f32_16x16x32_bf16 v[92:95], a[224:227], a[128:131], v[92:95]\n"
    "v_mfma_f32_16x16x32_bf16 v[92:95], a[228:231], a[132:135], v[92:95]\n"
    "v_mfma_f32_16x16x32_bf16 v[92:95], a[232:235], a[136:139], v[92:95]\n"
    "v_mfma_f32_16x16x32_bf16 v[92:95], a[236:239], a[140:143], v[92:95]\n"
    "s_waitcnt lgkmcnt(0)\n"
    "v_mfma_f32_16x16x32_bf16 v[0:3], a[144:147], a[0:3], 0\n"
    "v_cvt_pk_bf16_f32 v100, v48, v49\n"
    "v_mfma_f32_16x16x32_bf16 v[0:3], a[148:151], a[4:7], v[0:3]\n"
    "s_add_u32 s70, s62, s56\n"
    "v_mfma_f32_16x16x32_bf16 v[0:3], a[152:155], a[8:11], v[0:3]\n"
    "v_cvt_pk_bf16_f32 v101, v50, v51\n"
    "v_mfma_f32_16x16x32_bf16 v[0:3], a[156:159], a[12:15], v[0:3]\n"
    "s_add_u32 m0, s70, 0\n"
    "s_add_u32 s73, s52, 0\n"
    "v_mfma_f32_16x16x32_bf16 v[0:3], a[160:163], a[16:19], v[0:3]\n"
    "buffer_load_dwordx4 v228, s[20:23], s73 offen nt lds\n"
    "v_mfma_f32_16x16x32_bf16 v[0:3], a[164:167], a[20:23], v[0:3]\n"
    "v_cvt_pk_bf16_f32 v108, v52, v53\n"
    "v_mfma_f32_16x16x32_bf16 v[4:7], a[144:147], a[24:27], 0\n"
    "v_cvt_pk_bf16_f32 v109, v54, v55\n"
    "v_mfma_f32_16x16x32_bf16 v[4:7], a[148:151], a[28:31], v[4:7]\n"
    "v_cvt_pk_bf16_f32 v102, v72, v73\n"
    "v_mfma_f32_16x16x32_bf16 v[4:7], a[152:155], a[32:35], v[4:7]\n"
    "buffer_load_dwordx4 v229, s[20:23], s73 offen offset:1024 nt lds\n"
    "v_mfma_f32_16x16x32_bf16 v[4:7], a[156:159], a[36:39], v[4:7]\n"
    "v_cvt_pk_bf16_f32 v103, v74, v75\n"
    "v_mfma_f32_16x16x32_bf16 v[4:7], a[160:163], a[40:43], v[4:7]\n"
    "v_cvt_pk_bf16_f32 v110, v76, v77\n"
    "v_mfma_f32_16x16x32_bf16 v[4:7], a[164:167], a[44:47], v[4:7]\n"
    "v_cvt_pk_bf16_f32 v111, v78, v79\n"
    "v_mfma_f32_16x16x32_bf16 v[24:27], a[168:171], a[0:3], 0\n"
    "buffer_load_dwordx4 v230, s[20:23], s73 offen offset:2048 nt lds\n"
    "v_mfma_f32_16x16x32_bf16 v[24:27], a[172:175], a[4:7], v[24:27]\n"
    "ds_read_b128 a[192:195], v235 offset:12288\n"
    "v_mfma_f32_16x16x32_bf16 v[24:27], a[176:179], a[8:11], v[24:27]\n"
    "ds_read_b128 a[196:199], v236 offset:12288\n"
    "v_mfma_f32_16x16x32_bf16 v[24:27], a[180:183], a[12:15], v[24:27]\n"
    "s_add_u32 m0, s70, 6144\n"
    "s_add_u32 s73, s52, 294912\n"
    "ds_read_b128 a[200:203], v235 offset:12416\n"
    "ds_read_b128 a[204:207], v236 offset:12416\n"
    "ds_read_b128 a[208:211], v235 offset:12544\n"
    "ds_read_b128 a[212:215], v236 offset:12544\n"
    "v_mfma_f32_16x16x32_bf16 v[24:27], a[184:187], a[16:19], v[24:27]\n"
    "buffer_load_dwordx4 v228, s[20:23], s73 offen nt lds\n"
    "v_mfma_f32_16x16x32_bf16 v[24:27], a[188:191], a[20:23], v[24:27]\n"
    "v_cvt_pk_bf16_f32 v112, v8, v9\n"
    "v_mfma_f32_16x16x32_bf16 v[28:31], a[168:171], a[24:27], 0\n"
    "v_cvt_pk_bf16_f32 v113, v10, v11\n"
    "v_mfma_f32_16x16x32_bf16 v[28:31], a[172:175], a[28:31], v[28:31]\n"
    "v_cvt_pk_bf16_f32 v120, v12, v13\n"
    "v_mfma_f32_16x16x32_bf16 v[28:31], a[176:179], a[32:35], v[28:31]\n"
    "buffer_load_dwordx4 v229, s[20:23], s73 offen offset:1024 nt lds\n"
    "v_mfma_f32_16x16x32_bf16 v[28:31], a[180:183], a[36:39], v[28:31]\n"
    "v_cvt_pk_bf16_f32 v121, v14, v15\n"
    "v_mfma_f32_16x16x32_bf16 v[28:31], a[184:187], a[40:43], v[28:31]\n"
    "v_cvt_pk_bf16_f32 v114, v32, v33\n"
    "v_mfma_f32_16x16x32_bf16 v[28:31], a[188:191], a[44:47], v[28:31]\n"
    "v_cvt_pk_bf16_f32 v115, v34, v35\n"
    "s_waitcnt lgkmcnt(5)\n"
    "v_mfma_f32_16x16x32_bf16 v[48:51], a[192:195], a[0:3], 0\n"
    "buffer_load_dwordx4 v230, s[20:23], s73 offen offset:2048 nt lds\n"
    "s_waitcnt lgkmcnt(4)\n"
    "v_mfma_f32_16x16x32_bf16 v[48:51], a[196:199], a[4:7], v[48:51]\n"
    "ds_read_b128 a[216:219], v235 offset:18432\n"
    "s_waitcnt lgkmcnt(1)\n"
    "v_mfma_f32_16x16x32_bf16 v[48:51], a[200:203], a[8:11], v[48:51]\n"
    "ds_read_b128 a[220:223], v236 offset:18432\n"
    "v_mfma_f32_16x16x32_bf16 v[48:51], a[204:207], a[12:15], v[48:51]\n"
    "s_add_u32 m0, s70, 12288\n"
    "s_add_u32 s73, s52, 589824\n"
    "ds_read_b128 a[224:227], v235 offset:18560\n"
    "ds_read_b128 a[228:231], v236 offset:18560\n"
    "ds_read_b128 a[232:235], v235 offset:18688\n"
    "ds_read_b128 a[236:239], v236 offset:18688\n"
    "v_mfma_f32_16x16x32_bf16 v[48:51], a[208:211], a[16:19], v[48:51]\n"
    "buffer_load_dwordx4 v228, s[20:23], s73 offen nt lds\n"
    "v_mfma_f32_16x16x32_bf16 v[48:51], a[212:215], a[20:23], v[48:51]\n"
    "v_cvt_pk_bf16_f32 v122, v36, v37\n"
    "v_mfma_f32_16x16x32_bf16 v[52:55], a[192:195], a[24:27], 0\n"
    "v_cvt_pk_bf16_f32 v123, v38, v39\n"
    "v_mfma_f32_16x16x32_bf16 v[52:55], a[196:199], a[28:31], v[52:55]\n"
    "v_cvt_pk_bf16_f32 v116, v56, v57\n"
    "v_mfma_f32_16x16x32_bf16 v[52:55], a[200:203], a[32:35], v[52:55]\n"
    "buffer_load_dwordx4 v229, s[20:23], s73 offen offset:1024 nt lds\n"
    "v_mfma_f32_16x16x32_bf16 v[52:55], a[204:207], a[36:39], v[52:55]\n"
    "v_cvt_pk_bf16_f32 v117, v58, v59\n"
    "v_mfma_f32_16x16x32_bf16 v[52:55], a[208:211], a[40:43], v[52:55]\n"
    "v_cvt_pk_bf16_f32 v124, v60, v61\n"
    "v_mfma_f32_16x16x32_bf16 v[52:55], a[212:215], a[44:47], v[52:55]\n"
    "v_cvt_pk_bf16_f32 v125, v62, v63\n"
    "s_waitcnt lgkmcnt(5)\n"
    "v_mfma_f32_16x16x32_bf16 v[72:75], a[216:219], a[0:3], 0\n"
    "buffer_load_dwordx4 v230, s[20:23], s73 offen offset:2048 nt lds\n"
    "s_waitcnt lgkmcnt(4)\n"
    "v_mfma_f32_16x16x32_bf16 v[72:75], a[220:223], a[4:7], v[72:75]\n"
    "v_cvt_pk_bf16_f32 v118, v80, v81\n"
    "s_waitcnt lgkmcnt(0)\n"
    "v_mfma_f32_16x16x32_bf16 v[72:75], a[224:227], a[8:11], v[72:75]\n"
    "v_cvt_pk_bf16_f32 v119, v82, v83\n"
    "v_mfma_f32_16x16x32_bf16 v[72:75], a[228:231], a[12:15], v[72:75]\n"
    "s_add_u32 m0, s70, 18432\n"
    "s_add_u32 s73, s52, 884736\n"
    "v_mfma_f32_16x16x32_bf16 v[72:75], a[232:235], a[16:19], v[72:75]\n"
    "buffer_load_dwordx4 v228, s[20:23], s73 offen nt lds\n"
    "v_mfma_f32_16x16x32_bf16 v[72:75], a[236:239], a[20:23], v[72:75]\n"
    "v_cvt_pk_bf16_f32 v126, v84, v85\n"
    "v_mfma_f32_16x16x32_bf16 v[76:79], a[216:219], a[24:27], 0\n"
    "v_cvt_pk_bf16_f32 v127, v86, v87\n"
    "v_mfma_f32_16x16x32_bf16 v[76:79], a[220:223], a[28:31], v[76:79]\n"
    "v_cvt_pk_bf16_f32 v128, v16, v17\n"
    "v_mfma_f32_16x16x32_bf16 v[76:79], a[224:227], a[32:35], v[76:79]\n"
    "buffer_load_dwordx4 v229, s[20:23], s73 offen offset:1024 nt lds\n"
    "v_mfma_f32_16x16x32_bf16 v[76:79], a[228:231], a[36:39], v[76:79]\n"
    "v_cvt_pk_bf16_f32 v129, v18, v19\n"
    "v_mfma_f32_16x16x32_bf16 v[76:79], a[232:235], a[40:43], v[76:79]\n"
    "v_cvt_pk_bf16_f32 v136, v20, v21\n"
    "v_mfma_f32_16x16x32_bf16 v[76:79], a[236:239], a[44:47], v[76:79]\n"
    "v_cvt_pk_bf16_f32 v137, v22, v23\n"
    "v_mfma_f32_16x16x32_bf16 v[8:11], a[144:147], a[48:51], 0\n"
    "buffer_load_dwordx4 v230, s[20:23], s73 offen offset:2048 nt lds\n"
    "v_mfma_f32_16x16x32_bf16 v[8:11], a[148:151], a[52:55], v[8:11]\n"
    "s_add_u32 s52, s52, 5947392\n"
    "s_add_u32 s53, s53, 16\n"
    "v_mfma_f32_16x16x32_bf16 v[8:11], a[152:155], a[56:59], v[8:11]\n"
    "s_cmp_ge_u32 s53, 48\n"
    "s_cselect_b32 s66, 1032192, 0\n"
    "s_cselect_b32 s67, 48, 0\n"
    "v_mfma_f32_16x16x32_bf16 v[8:11], a[156:159], a[60:63], v[8:11]\n"
    "s_add_u32 s52, s52, s66\n"
    "s_sub_u32 s53, s53, s67\n"
    "v_mfma_f32_16x16x32_bf16 v[8:11], a[160:163], a[64:67], v[8:11]\n"
    "s_add_u32 s62, s62, 24576\n"
    "s_cmp_eq_u32 s62, 122880\n"
    "s_cselect_b32 s62, 0, s62\n"
    "v_mfma_f32_16x16x32_bf16 v[8:11], a[164:167], a[68:71], v[8:11]\n"
    "s_add_u32 s70, s62, s56\n"
    "v_mfma_f32_16x16x32_bf16 v[12:15], a[144:147], a[72:75], 0\n"
    "v_cvt_pk_bf16_f32 v130, v40, v41\n"
    "v_mfma_f32_16x16x32_bf16 v[12:15], a[148:151], a[76:79], v[12:15]\n"
    "s_add_u32 m0, s70, 0\n"
    "s_add_u32 s73, s52, 0\n"
    "v_mfma_f32_16x16x32_bf16 v[12:15], a[152:155], a[80:83], v[12:15]\n"
    "buffer_load_dwordx4 v228, s[20:23], s73 offen nt lds\n"
    "v_mfma_f32_16x16x32_bf16 v[12:15], a[156:159], a[84:87], v[12:15]\n"
    "v_cvt_pk_bf16_f32 v131, v42, v43\n"
    "v_mfma_f32_16x16x32_bf16 v[12:15], a[160:163], a[88:91], v[12:15]\n"
    "v_cvt_pk_bf16_f32 v138, v44, v45\n"
    "v_mfma_f32_16x16x32_bf16 v[12:15], a[164:167], a[92:95], v[12:15]\n"
    "v_cvt_pk_bf16_f32 v139, v46, v47\n"
    "v_mfma_f32_16x16x32_bf16 v[32:35], a[168:171], a[48:51], 0\n"
    "v_cvt_pk_bf16_f32 v132, v64, v65\n"
    "v_mfma_f32_16x16x32_bf16 v[32:35], a[172:175], a[52:55], v[32:35]\n"
    "buffer_load_dwordx4 v229, s[20:23], s73 offen offset:1024 nt lds\n"
    "v_mfma_f32_16x16x32_bf16 v[32:35], a[176:179], a[56:59], v[32:35]\n"
    "v_cvt_pk_bf16_f32 v133, v66, v67\n"
    "v_mfma_f32_16x16x32_bf16 v[32:35], a[180:183], a[60:63], v[32:35]\n"
    "v_cvt_pk_bf16_f32 v140, v68, v69\n"
    "v_mfma_f32_16x16x32_bf16 v[32:35], a[184:187], a[64:67], v[32:35]\n"
    "v_cvt_pk_bf16_f32 v141, v70, v71\n"
    "v_mfma_f32_16x16x32_bf16 v[32:35], a[188:191], a[68:71], v[32:35]\n"
    "v_cvt_pk_bf16_f32 v134, v88, v89\n"
    "v_mfma_f32_16x16x32_bf16 v[36:39], a[168:171], a[72:75], 0\n"
    "buffer_load_dwordx4 v230, s[20:23], s73 offen offset:2048 nt lds\n"
    "v_mfma_f32_16x16x32_bf16 v[36:39], a[172:175], a[76:79], v[36:39]\n"
    "v_cvt_pk_bf16_f32 v135, v90, v91\n"
    "v_mfma_f32_16x16x32_bf16 v[36:39], a[176:179], a[80:83], v[36:39]\n"
    "v_cvt_pk_bf16_f32 v142, v92, v93\n"
    "v_mfma_f32_16x16x32_bf16 v[36:39], a[180:183], a[84:87], v[36:39]\n"
    "v_cvt_pk_bf16_f32 v143, v94, v95\n"
    "v_mfma_f32_16x16x32_bf16 v[36:39], a[184:187], a[88:91], v[36:39]\n"
    "s_add_u32 m0, s70, 6144\n"
    "s_add_u32 s73, s52, 294912\n"
    "v_mfma_f32_16x16x32_bf16 v[36:39], a[188:191], a[92:95], v[36:39]\n"
    "buffer_load_dwordx4 v228, s[20:23], s73 offen nt lds\n"
    "v_mfma_f32_16x16x32_bf16 v[56:59], a[192:195], a[48:51], 0\n"
    "v_mfma_f32_16x16x32_bf16 v[56:59], a[196:199], a[52:55], v[56:59]\n"
    "v_mfma_f32_16x16x32_bf16 v[56:59], a[200:203], a[56:59], v[56:59]\n"
    "v_mfma_f32_16x16x32_bf16 v[56:59], a[204:207], a[60:63], v[56:59]\n"
    "v_mfma_f32_16x16x32_bf16 v[56:59], a[208:211], a[64:67], v[56:59]\n"
    "buffer_load_dwordx4 v229, s[20:23], s73 offen offset:1024 nt lds\n"
    "v_mfma_f32_16x16x32_bf16 v[56:59], a[212:215], a[68:71], v[56:59]\n"
    "v_mfma_f32_16x16x32_bf16 v[60:63], a[192:195], a[72:75], 0\n"
    "v_mfma_f32_16x16x32_bf16 v[60:63], a[196:199], a[76:79], v[60:63]\n"
    "v_mfma_f32_16x16x32_bf16 v[60:63], a[200:203], a[80:83], v[60:63]\n"
    "v_mfma_f32_16x16x32_bf16 v[60:63], a[204:207], a[84:87], v[60:63]\n"
    "buffer_load_dwordx4 v230, s[20:23], s73 offen offset:2048 nt lds\n"
    "v_mfma_f32_16x16x32_bf16 v[60:63], a[208:211], a[88:91], v[60:63]\n"
    "v_mfma_f32_16x16x32_bf16 v[60:63], a[212:215], a[92:95], v[60:63]\n"
    "v_mfma_f32_16x16x32_bf16 v[80:83], a[216:219], a[48:51], 0\n"
    "v_mfma_f32_16x16x32_bf16 v[80:83], a[220:223], a[52:55], v[80:83]\n"
    "s_add_u32 m0, s70, 12288\n"
    "s_add_u32 s73, s52, 589824\n"
    "v_mfma_f32_16x16x32_bf16 v[80:83], a[224:227], a[56:59], v[80:83]\n"
    "buffer_load_dwordx4 v228, s[20:23], s73 offen nt lds\n"
    "s_barrier\n"
    "v_mfma_f32_16x16x32_bf16 v[80:83], a[228:231], a[60:63], v[80:83]\n"
    "v_mfma_f32_16x16x32_bf16 v[80:83], a[232:235], a[64:67], v[80:83]\n"
    "v_mfma_f32_16x16x32_bf16 v[80:83], a[236:239], a[68:71], v[80:83]\n"
    "v_mfma_f32_16x16x32_bf16 v[84:87], a[216:219], a[72:75], 0\n"
    "v_mfma_f32_16x16x32_bf16 v[84:87], a[220:223], a[76:79], v[84:87]\n"
    "buffer_load_dwordx4 v229, s[20:23], s73 offen offset:1024 nt lds\n"
    "v_mfma_f32_16x16x32_bf16 v[84:87], a[224:227], a[80:83], v[84:87]\n"
    "v_mfma_f32_16x16x32_bf16 v[84:87], a[228:231], a[84:87], v[84:87]\n"
    "v_mfma_f32_16x16x32_bf16 v[84:87], a[232:235], a[88:91], v[84:87]\n"
    "v_mfma_f32_16x16x32_bf16 v[84:87], a[236:239], a[92:95], v[84:87]\n"
    "v_mfma_f32_16x16x32_bf16 v[16:19], a[144:147], a[96:99], 0\n"
    "buffer_load_dwordx4 v230, s[20:23], s73 offen offset:2048 nt lds\n"
    "v_mfma_f32_16x16x32_bf16 v[16:19], a[148:151], a[100:103], v[16:19]\n"
    "v_mfma_f32_16x16x32_bf16 v[16:19], a[152:155], a[104:107], v[16:19]\n"
    "v_mfma_f32_16x16x32_bf16 v[16:19], a[156:159], a[108:111], v[16:19]\n"
    "v_mfma_f32_16x16x32_bf16 v[16:19], a[160:163], a[112:115], v[16:19]\n"
    "s_add_u32 m0, s70, 18432\n"
    "s_add_u32 s73, s52, 884736\n"
    "v_mfma_f32_16x16x32_bf16 v[16:19], a[164:167], a[116:119], v[16:19]\n"
    "buffer_load_dwordx4 v228, s[20:23], s73 offen nt lds\n"
    "v_mfma_f32_16x16x32_bf16 v[20:23], a[144:147], a[120:123], 0\n"
    "v_mfma_f32_16x16x32_bf16 v[20:23], a[148:151], a[124:127], v[20:23]\n"
    "v_mfma_f32_16x16x32_bf16 v[20:23], a[152:155], a[128:131], v[20:23]\n"
    "v_mfma_f32_16x16x32_bf16 v[20:23], a[156:159], a[132:135], v[20:23]\n"
    "v_mfma_f32_16x16x32_bf16 v[20:23], a[160:163], a[136:139], v[20:23]\n"
    "buffer_load_dwordx4 v229, s[20:23], s73 offen offset:1024 nt lds\n"
    "v_mfma_f32_16x16x32_bf16 v[20:23], a[164:167], a[140:143], v[20:23]\n"
    "v_mfma_f32_16x16x32_bf16 v[40:43], a[168:171], a[96:99], 0\n"
    "v_mfma_f32_16x16x32_bf16 v[40:43], a[172:175], a[100:103], v[40:43]\n"
    "v_mfma_f32_16x16x32_bf16 v[40:43], a[176:179], a[104:107], v[40:43]\n"
    "v_mfma_f32_16x16x32_bf16 v[40:43], a[180:183], a[108:111], v[40:43]\n"
    "buffer_load_dwordx4 v230, s[20:23], s73 offen offset:2048 nt lds\n"
    "v_mfma_f32_16x16x32_bf16 v[40:43], a[184:187], a[112:115], v[40:43]\n"
    "s_add_u32 s52, s52, 5947392\n"
    "s_add_u32 s53, s53, 16\n"
    "v_mfma_f32_16x16x32_bf16 v[40:43], a[188:191], a[116:119], v[40:43]\n"
    "s_cmp_ge_u32 s53, 48\n"
    "s_cselect_b32 s66, 1032192, 0\n"
    "s_cselect_b32 s67, 48, 0\n"
    "v_mfma_f32_16x16x32_bf16 v[44:47], a[168:171], a[120:123], 0\n"
    "s_add_u32 s52, s52, s66\n"
    "s_sub_u32 s53, s53, s67\n"
    "v_mfma_f32_16x16x32_bf16 v[44:47], a[172:175], a[124:127], v[44:47]\n"
    "s_add_u32 s62, s62, 24576\n"
    "s_cmp_eq_u32 s62, 122880\n"
    "s_cselect_b32 s62, 0, s62\n"
    "v_mfma_f32_16x16x32_bf16 v[44:47], a[176:179], a[128:131], v[44:47]\n"
    "v_mfma_f32_16x16x32_bf16 v[44:47], a[180:183], a[132:135], v[44:47]\n"
    "v_mfma_f32_16x16x32_bf16 v[44:47], a[184:187], a[136:139], v[44:47]\n"
    "v_mfma_f32_16x16x32_bf16 v[44:47], a[188:191], a[140:143], v[44:47]\n"
    "s_waitcnt vmcnt(24)\n"
    "s_add_u32 s60, s60, 1\n"
    "s_waitcnt lgkmcnt(0)\n"
    "s_barrier\n"
    ".Lw_B1:\n"
    "ds_read_b128 a[240:243], v238\n"
    "ds_read_b128 a[244:247], v238 offset:1024\n"
    "v_mfma_f32_16x16x32_bf16 v[64:67], a[192:195], a[96:99], 0\n"
    "ds_read_b128 a[248:251], v238 offset:2048\n"
    "ds_read_b128 a[252:255], v238 offset:3072\n"
    "v_mfma_f32_16x16x32_bf16 v[64:67], a[196:199], a[100:103], v[64:67]\n"
    "ds_read_b128 v[176:179], v238 offset:4096\n"
    "ds_read_b128 v[180:183], v238 offset:5120\n"
    "v_mfma_f32_16x16x32_bf16 v[64:67], a[200:203], a[104:107], v[64:67]\n"
    "ds_read_b128 v[184:187], v238 offset:6144\n"
    "ds_read_b128 v[188:191], v238 offset:7168\n"
    "v_mfma_f32_16x16x32_bf16 v[64:67], a[204:207], a[108:111], v[64:67]\n"
    "s_mov_b32 s64, s63\n"
    "s_mov_b32 s63, s61\n"
    "v_mfma_f32_16x16x32_bf16 v[64:67], a[208:211], a[112:115], v[64:67]\n"
    "s_add_u32 s61, s61, 24576\n"
    "s_cmp_eq_u32 s61, 122880\n"
    "s_cselect_b32 s61, 0, s61\n"
    "v_mfma_f32_16x16x32_bf16 v[64:67], a[212:215], a[116:119], v[64:67]\n"
    "s_sub_u32 s63, s61, s63\n"
    "v_add_u32 v235, s63, v235\n"
    "v_mfma_f32_16x16x32_bf16 v[68:71], a[192:195], a[120:123], 0\n"
    "v_add_u32 v236, s63, v236\n"
    "v_add_u32 v237, s64, v237\n"
    "v_mfma_f32_16x16x32_bf16 v[68:71], a[196:199], a[124:127], v[68:71]\n"
    "v_add_u32 v239, s64, v239\n"
    "v_add_u32 v238, s77, v238\n"
    "s_sub_u32 s77, 0, s77\n"
    "v_mfma_f32_16x16x32_bf16 v[68:71], a[200:203], a[128:131], v[68:71]\n"
    "ds_read_b128 a[144:147], v235\n"
    "v_mfma_f32_16x16x32_bf16 v[68:71], a[204:207], a[132:135], v[68:71]\n"
    "ds_read_b128 a[148:151], v236\n"
    "ds_read_b128 a[152:155], v235 offset:128\n"
    "v_mfma_f32_16x16x32_bf16 v[68:71], a[208:211], a[136:139], v[68:71]\n"
    "ds_read_b128 a[156:159], v236 offset:128\n"
    "v_mfma_f32_16x16x32_bf16 v[68:71], a[212:215], a[140:143], v[68:71]\n"
    "ds_read_b128 a[160:163], v235 offset:256\n"
    "ds_read_b128 a[164:167], v236 offset:256\n"
    "s_waitcnt lgkmcnt(8)\n"
    "v_mfma_f32_16x16x32_bf16 v[144:147], v[96:99], a[240:243], 0\n"
    "ds_read_b128 a[168:171], v235 offset:6144\n"
    "v_mfma_f32_16x16x32_bf16 v[144:147], v[100:103], a[244:247], v[144:147]\n"
    "ds_read_b128 a[172:175], v236 offset:6144\n"
    "ds_read_b128 a[176:179], v235 offset:6272\n"
    "v_mfma_f32_16x16x32_bf16 v[148:151], v[104:107], a[240:243], 0\n"
    "ds_read_b128 a[180:183], v236 offset:6272\n"
    "v_mfma_f32_16x16x32_bf16 v[148:151], v[108:111], a[244:247], v[148:151]\n"
    "ds_read_b128 a[184:187], v235 offset:6400\n"
    "ds_read_b128 a[188:191], v236 offset:6400\n"
    "v_mfma_f32_16x16x32_bf16 v[152:155], v[96:99], a[248:251], 0\n"
    "v_mfma_f32_16x16x32_bf16 v[152:155], v[100:103], a[252:255], v[152:155]\n"
    "v_mfma_f32_16x16x32_bf16 v[156:159], v[104:107], a[248:251], 0\n"
    "v_mfma_f32_16x16x32_bf16 v[156:159], v[108:111], a[252:255], v[156:159]\n"
    "v_mfma_f32_16x16x32_bf16 v[88:91], a[216:219], a[96:99], 0\n"
    "v_cvt_pk_bf16_f32 v192, v144, v145\n"
    "v_mfma_f32_16x16x32_bf16 v[88:91], a[220:223], a[100:103], v[88:91]\n"
    "v_cvt_pk_bf16_f32 v193, v146, v147\n"
    "v_mfma_f32_16x16x32_bf16 v[88:91], a[224:227], a[104:107], v[88:91]\n"
    "ds_write_b64 v237, v[192:193]\n"
    "v_mfma_f32_16x16x32_bf16 v[88:91], a[228:231], a[108:111], v[88:91]\n"
    "v_cvt_pk_bf16_f32 v194, v148, v149\n"
    "v_cvt_pk_bf16_f32 v195, v150, v151\n"
    "v_mfma_f32_16x16x32_bf16 v[88:91], a[232:235], a[112:115], v[88:91]\n"
    "ds_write_b64 v237, v[194:195] offset:8\n"
    "v_mfma_f32_16x16x32_bf16 v[88:91], a[236:239], a[116:119], v[88:91]\n"
    "v_cvt_pk_bf16_f32 v196, v152, v153\n"
    "v_cvt_pk_bf16_f32 v197, v154, v155\n"
    "v_mfma_f32_16x16x32_bf16 v[160:163], v[96:99], v[176:179], 0\n"
    "ds_write_b64 v237, v[196:197] offset:6144\n"
    "v_mfma_f32_16x16x32_bf16 v[160:163], v[100:103], v[180:183], v[160:163]\n"
    "v_cvt_pk_bf16_f32 v198, v156, v157\n"
    "v_cvt_pk_bf16_f32 v199, v158, v159\n"
    "v_mfma_f32_16x16x32_bf16 v[164:167], v[104:107], v[176:179], 0\n"
    "ds_write_b64 v237, v[198:199] offset:6152\n"
    "v_mfma_f32_16x16x32_bf16 v[164:167], v[108:111], v[180:183], v[164:167]\n"
    "v_mfma_f32_16x16x32_bf16 v[92:95], a[216:219], a[120:123], 0\n"
    "v_mfma_f32_16x16x32_bf16 v[92:95], a[220:223], a[124:127], v[92:95]\n"
    "v_mfma_f32_16x16x32_bf16 v[92:95], a[224:227], a[128:131], v[92:95]\n"
    "v_mfma_f32_16x16x32_bf16 v[92:95], a[228:231], a[132:135], v[92:95]\n"
    "v_mfma_f32_16x16x32_bf16 v[92:95], a[232:235], a[136:139], v[92:95]\n"
    "v_cvt_pk_bf16_f32 v200, v160, v161\n"
    "v_mfma_f32_16x16x32_bf16 v[92:95], a[236:239], a[140:143], v[92:95]\n"
    "v_cvt_pk_bf16_f32 v201, v162, v163\n"
    "s_waitcnt lgkmcnt(2)\n"
    "v_mfma_f32_16x16x32_bf16 v[168:171], v[96:99], v[184:187], 0\n"
    "ds_write_b64 v237, v[200:201] offset:12288\n"
    "v_mfma_f32_16x16x32_bf16 v[168:171], v[100:103], v[188:191], v[168:171]\n"
    "v_cvt_pk_bf16_f32 v202, v164, v165\n"
    "v_cvt_pk_bf16_f32 v203, v166, v167\n"
    "v_mfma_f32_16x16x32_bf16 v[172:175], v[104:107], v[184:187], 0\n"
    "ds_write_b64 v237, v[202:203] offset:12296\n"
    "v_mfma_f32_16x16x32_bf16 v[172:175], v[108:111], v[188:191], v[172:175]\n"
    "s_cmp_ge_u32 s60, 9\n"
    "s_cbranch_scc1 .Lw_EPIB\n"
    ".Lw_B2:\n"
    "v_mfma_f32_16x16x32_bf16 v[144:147], v[112:115], a[240:243], 0\n"
    "v_cvt_pk_bf16_f32 v204, v168, v169\n"
    "v_mfma_f32_16x16x32_bf16 v[144:147], v[116:119], a[244:247], v[144:147]\n"
    "v_cvt_pk_bf16_f32 v205, v170, v171\n"
    "v_cvt_pk_bf16_f32 v96, v0, v1\n"
    "v_mfma_f32_16x16x32_bf16 v[148:151], v[120:123], a[240:243], 0\n"
    "ds_write_b64 v237, v[204:205] offset:18432\n"
    "v_mfma_f32_16x16x32_bf16 v[148:151], v[124:127], a[244:247], v[148:151]\n"
    "v_cvt_pk_bf16_f32 v97, v2, v3\n"
    "v_cvt_pk_bf16_f32 v206, v172, v173\n"
    "v_mfma_f32_16x16x32_bf16 v[152:155], v[112:115], a[248:251], 0\n"
    "s_add_u32 s70, s62, s56\n"
    "v_mfma_f32_16x16x32_bf16 v[152:155], v[116:119], a[252:255], v[152:155]\n"
    "s_cmp_lt_u32 s60, 6\n"
    "s_cselect_b32 s72, s52, 56623104\n"
    "v_cvt_pk_bf16_f32 v207, v174, v175\n"
    "v_mfma_f32_16x16x32_bf16 v[156:159], v[120:123], a[248:251], 0\n"
    "s_add_u32 m0, s70, 0\n"
    "s_add_u32 s73, s72, 0\n"
    "v_mfma_f32_16x16x32_bf16 v[156:159], v[124:127], a[252:255], v[156:159]\n"
    "buffer_load_dwordx4 v228, s[20:23], s73 offen nt lds\n"
    "v_cvt_pk_bf16_f32 v104, v4, v5\n"
    "v_mfma_f32_16x16x32_bf16 v[0:3], a[144:147], a[0:3], 0\n"
    "ds_write_b64 v237, v[206:207] offset:18440\n"
    "v_mfma_f32_16x16x32_bf16 v[0:3], a[148:151], a[4:7], v[0:3]\n"
    "v_cvt_pk_bf16_f32 v192, v144, v145\n"
    "v_cvt_pk_bf16_f32 v193, v146, v147\n"
    "v_mfma_f32_16x16x32_bf16 v[0:3], a[152:155], a[8:11], v[0:3]\n"
    "ds_write_b64 v237, v[192:193] offset:128\n"
    "v_mfma_f32_16x16x32_bf16 v[0:3], a[156:159], a[12:15], v[0:3]\n"
    "v_cvt_pk_bf16_f32 v194, v148, v149\n"
    "v_cvt_pk_bf16_f32 v195, v150, v151\n"
    "v_mfma_f32_16x16x32_bf16 v[0:3], a[160:163], a[16:19], v[0:3]\n"
    "ds_write_b64 v237, v[194:195] offset:136\n"
    "v_mfma_f32_16x16x32_bf16 v[0:3], a[164:167], a[20:23], v[0:3]\n"
    "buffer_load_dwordx4 v229, s[20:23], s73 offen offset:1024 nt lds\n"
    "v_cvt_pk_bf16_f32 v105, v6, v7\n"
    "v_mfma_f32_16x16x32_bf16 v[160:163], v[112:115], v[176:179], 0\n"
    "v_cvt_pk_bf16_f32 v196, v152, v153\n"
    "v_mfma_f32_16x16x32_bf16 v[160:163], v[116:119], v[180:183], v[160:163]\n"
    "v_cvt_pk_bf16_f32 v197, v154, v155\n"
    "v_cvt_pk_bf16_f32 v98, v24, v25\n"
    "v_mfma_f32_16x16x32_bf16 v[164:167], v[120:123], v[176:179], 0\n"
    "ds_write_b64 v237, v[196:197] offset:6272\n"
    "v_mfma_f32_16x16x32_bf16 v[164:167], v[124:127], v[180:183], v[164:167]\n"
    "v_cvt_pk_bf16_f32 v198, v156, v157\n"
    "v_cvt_pk_bf16_f32 v199, v158, v159\n"
    "v_mfma_f32_16x16x32_bf16 v[4:7], a[144:147], a[24:27], 0\n"
    "ds_write_b64 v237, v[198:199] offset:6280\n"
    "v_mfma_f32_16x16x32_bf16 v[4:7], a[148:151], a[28:31], v[4:7]\n"
    "buffer_load_dwordx4 v230, s[20:23], s73 offen offset:2048 nt lds\n"
    "v_cvt_pk_bf16_f32 v99, v26, v27\n"
    "v_mfma_f32_16x16x32_bf16 v[4:7], a[152:155], a[32:35], v[4:7]\n"
    "v_cvt_pk_bf16_f32 v106, v28, v29\n"
    "v_mfma_f32_16x16x32_bf16 v[4:7], a[156:159], a[36:39], v[4:7]\n"
    "v_cvt_pk_bf16_f32 v107, v30, v31\n"
    "v_cvt_pk_bf16_f32 v100, v48, v49\n"
    "v_mfma_f32_16x16x32_bf16 v[4:7], a[160:163], a[40:43], v[4:7]\n"
    "v_cvt_pk_bf16_f32 v200, v160, v161\n"
    "v_mfma_f32_16x16x32_bf16 v[4:7], a[164:167], a[44:47], v[4:7]\n"
    "v_cvt_pk_bf16_f32 v201, v162, v163\n"
    "v_cvt_pk_bf16_f32 v101, v50, v51\n"
    "v_mfma_f32_16x16x32_bf16 v[168:171], v[112:115], v[184:187], 0\n"
    "s_add_u32 m0, s70, 6144\n"
    "s_add_u32 s73, s72, 294912\n"
    "v_mfma_f32_16x16x32_bf16 v[168:171], v[116:119], v[188:191], v[168:171]\n"
    "buffer_load_dwordx4 v228, s[20:23], s73 offen nt lds\n"
    "v_cvt_pk_bf16_f32 v108, v52, v53\n"
    "v_mfma_f32_16x16x32_bf16 v[172:175], v[120:123], v[184:187], 0\n"
    "ds_write_b64 v237, v[200:201] offset:12416\n"
    "v_mfma_f32_16x16x32_bf16 v[172:175], v[124:127], v[188:191], v[172:175]\n"
    "v_cvt_pk_bf16_f32 v202, v164, v165\n"
    "v_cvt_pk_bf16_f32 v203, v166, v167\n"
    "v_mfma_f32_16x16x32_bf16 v[24:27], a[168:171], a[0:3], 0\n"
    "ds_write_b64 v237, v[202:203] offset:12424\n"
    "v_mfma_f32_16x16x32_bf16 v[24:27], a[172:175], a[4:7], v[24:27]\n"
    "v_cvt_pk_bf16_f32 v109, v54, v55\n"
    "v_cvt_pk_bf16_f32 v102, v72, v73\n"
    "v_mfma_f32_16x16x32_bf16 v[24:27], a[176:179], a[8:11], v[24:27]\n"
    "v_cvt_pk_bf16_f32 v103, v74, v75\n"
    "v_mfma_f32_16x16x32_bf16 v[24:27], a[180:183], a[12:15], v[24:27]\n"
    "buffer_load_dwordx4 v229, s[20:23], s73 offen offset:1024 nt lds\n"
    "v_cvt_pk_bf16_f32 v110, v76, v77\n"
    "v_mfma_f32_16x16x32_bf16 v[24:27], a[184:187], a[16:19], v[24:27]\n"
    "v_cvt_pk_bf16_f32 v204, v168, v169\n"
    "v_mfma_f32_16x16x32_bf16 v[24:27], a[188:191], a[20:23], v[24:27]\n"
    "v_cvt_pk_bf16_f32 v205, v170, v171\n"
    "v_cvt_pk_bf16_f32 v111, v78, v79\n"
    "v_mfma_f32_16x16x32_bf16 v[144:147], v[128:131], a[240:243], 0\n"
    "ds_write_b64 v237, v[204:205] offset:18560\n"
    "v_mfma_f32_16x16x32_bf16 v[144:147], v[132:135], a[244:247], v[144:147]\n"
    "v_cvt_pk_bf16_f32 v206, v172, v173\n"
    "v_cvt_pk_bf16_f32 v207, v174, v175\n"
    "v_mfma_f32_16x16x32_bf16 v[148:151], v[136:139], a[240:243], 0\n"
    "ds_read_b128 a[192:195], v235 offset:12288\n"
    "v_mfma_f32_16x16x32_bf16 v[148:151], v[140:143], a[244:247], v[148:151]\n"
    "buffer_load_dwordx4 v230, s[20:23], s73 offen offset:2048 nt lds\n"
    "ds_read_b128 a[196:199], v236 offset:12288\n"
    "v_mfma_f32_16x16x32_bf16 v[28:31], a[168:171], a[24:27], 0\n"
    "ds_read_b128 a[200:203], v235 offset:12416\n"
    "v_mfma_f32_16x16x32_bf16 v[28:31], a[172:175], a[28:31], v[28:31]\n"
    "ds_read_b128 a[204:207], v236 offset:12416\n"
    "ds_read_b128 a[208:211], v235 offset:12544\n"
    "ds_read_b128 a[212:215], v236 offset:12544\n"
    "v_mfma_f32_16x16x32_bf16 v[28:31], a[176:179], a[32:35], v[28:31]\n"
    "ds_write_b64 v237, v[206:207] offset:18568\n"
    "v_mfma_f32_16x16x32_bf16 v[28:31], a[180:183], a[36:39], v[28:31]\n"
    "v_cvt_pk_bf16_f32 v112, v8, v9\n"
    "v_cvt_pk_bf16_f32 v113, v10, v11\n"
    "v_mfma_f32_16x16x32_bf16 v[28:31], a[184:187], a[40:43], v[28:31]\n"
    "s_add_u32 m0, s70, 12288\n"
    "s_add_u32 s73, s72, 589824\n"
    "v_mfma_f32_16x16x32_bf16 v[28:31], a[188:191], a[44:47], v[28:31]\n"
    "buffer_load_dwordx4 v228, s[20:23], s73 offen nt lds\n"
    "v_cvt_pk_bf16_f32 v192, v144, v145\n"
    "v_mfma_f32_16x16x32_bf16 v[152:155], v[128:131], a[248:251], 0\n"
    "v_cvt_pk_bf16_f32 v193, v146, v147\n"
    "v_mfma_f32_16x16x32_bf16 v[152:155], v[132:135], a[252:255], v[152:155]\n"
    "ds_write_b64 v237, v[192:193] offset:256\n"
    "v_mfma_f32_16x16x32_bf16 v[156:159], v[136:139], a[248:251], 0\n"
    "v_cvt_pk_bf16_f32 v194, v148, v149\n"
    "v_mfma_f32_16x16x32_bf16 v[156:159], v[140:143], a[252:255], v[156:159]\n"
    "v_cvt_pk_bf16_f32 v195, v150, v151\n"
    "v_cvt_pk_bf16_f32 v120, v12, v13\n"
    "s_waitcnt lgkmcnt(6)\n"
    "v_mfma_f32_16x16x32_bf16 v[48:51], a[192:195], a[0:3], 0\n"
    "ds_write_b64 v237, v[194:195] offset:264\n"
    "v_mfma_f32_16x16x32_bf16 v[48:51], a[196:199], a[4:7], v[48:51]\n"
    "buffer_load_dwordx4 v229, s[20:23], s73 offen offset:1024 nt lds\n"
    "v_cvt_pk_bf16_f32 v121, v14, v15\n"
    "s_waitcnt lgkmcnt(3)\n"
    "v_mfma_f32_16x16x32_bf16 v[48:51], a[200:203], a[8:11], v[48:51]\n"
    "v_cvt_pk_bf16_f32 v114, v32, v33\n"
    "v_mfma_f32_16x16x32_bf16 v[48:51], a[204:207], a[12:15], v[48:51]\n"
    "v_cvt_pk_bf16_f32 v115, v34, v35\n"
    "v_cvt_pk_bf16_f32 v122, v36, v37\n"
    "v_mfma_f32_16x16x32_bf16 v[48:51], a[208:211], a[16:19], v[48:51]\n"
    "v_cvt_pk_bf16_f32 v196, v152, v153\n"
    "v_mfma_f32_16x16x32_bf16 v[48:51], a[212:215], a[20:23], v[48:51]\n"
    "v_cvt_pk_bf16_f32 v197, v154, v155\n"
    "v_cvt_pk_bf16_f32 v123, v38, v39\n"
    "v_mfma_f32_16x16x32_bf16 v[160:163], v[128:131], v[176:179], 0\n"
    "ds_write_b64 v237, v[196:197] offset:6400\n"
    "v_mfma_f32_16x16x32_bf16 v[160:163], v[132:135], v[180:183], v[160:163]\n"
    "buffer_load_dwordx4 v230, s[20:23], s73 offen offset:2048 nt lds\n"
    "v_cvt_pk_bf16_f32 v198, v156, v157\n"
    "v_mfma_f32_16x16x32_bf16 v[164:167], v[136:139], v[176:179], 0\n"
    "ds_read_b128 a[216:219], v235 offset:18432\n"
    "v_mfma_f32_16x16x32_bf16 v[164:167], v[140:143], v[180:183], v[164:167]\n"
    "ds_read_b128 a[220:223], v236 offset:18432\n"
    "ds_read_b128 a[224:227], v235 offset:18560\n"
    "v_mfma_f32_16x16x32_bf16 v[52:55], a[192:195], a[24:27], 0\n"
    "ds_read_b128 a[228:231], v236 offset:18560\n"
    "v_mfma_f32_16x16x32_bf16 v[52:55], a[196:199], a[28:31], v[52:55]\n"
    "ds_read_b128 a[232:235], v235 offset:18688\n"
    "ds_read_b128 a[236:239], v236 offset:18688\n"
    "v_mfma_f32_16x16x32_bf16 v[52:55], a[200:203], a[32:35], v[52:55]\n"
    "s_add_u32 m0, s70, 18432\n"
    "s_add_u32 s73, s72, 884736\n"
    "v_mfma_f32_16x16x32_bf16 v[52:55], a[204:207], a[36:39], v[52:55]\n"
    "buffer_load_dwordx4 v228, s[20:23], s73 offen nt lds\n"
    "v_cvt_pk_bf16_f32 v199, v158, v159\n"
    "v_mfma_f32_16x16x32_bf16 v[52:55], a[208:211], a[40:43], v[52:55]\n"
    "ds_write_b64 v237, v[198:199] offset:6408\n"
    "v_mfma_f32_16x16x32_bf16 v[52:55], a[212:215], a[44:47], v[52:55]\n"
    "v_cvt_pk_bf16_f32 v200, v160, v161\n"
    "v_cvt_pk_bf16_f32 v201, v162, v163\n"
    "v_mfma_f32_16x16x32_bf16 v[168:171], v[128:131], v[184:187], 0\n"
    "ds_write_b64 v237, v[200:201] offset:12544\n"
    "v_mfma_f32_16x16x32_bf16 v[168:171], v[132:135], v[188:191], v[168:171]\n"
    "v_cvt_pk_bf16_f32 v202, v164, v165\n"
    "v_cvt_pk_bf16_f32 v203, v166, v167\n"
    "v_mfma_f32_16x16x32_bf16 v[172:175], v[136:139], v[184:187], 0\n"
    "ds_write_b64 v237, v[202:203] offset:12552\n"
    "v_mfma_f32_16x16x32_bf16 v[172:175], v[140:143], v[188:191], v[172:175]\n"
    "buffer_load_dwordx4 v229, s[20:23], s73 offen offset:1024 nt lds\n"
    "v_cvt_pk_bf16_f32 v116, v56, v57\n"
    "s_waitcnt lgkmcnt(6)\n"
    "v_mfma_f32_16x16x32_bf16 v[72:75], a[216:219], a[0:3], 0\n"
    "v_cvt_pk_bf16_f32 v117, v58, v59\n"
    "v_mfma_f32_16x16x32_bf16 v[72:75], a[220:223], a[4:7], v[72:75]\n"
    "v_cvt_pk_bf16_f32 v124, v60, v61\n"
    "v_cvt_pk_bf16_f32 v125, v62, v63\n"
    "v_mfma_f32_16x16x32_bf16 v[72:75], a[224:227], a[8:11], v[72:75]\n"
    "v_cvt_pk_bf16_f32 v118, v80, v81\n"
    "s_waitcnt lgkmcnt(3)\n"
    "v_mfma_f32_16x16x32_bf16 v[72:75], a[228:231], a[12:15], v[72:75]\n"
    "v_cvt_pk_bf16_f32 v119, v82, v83\n"
    "v_cvt_pk_bf16_f32 v126, v84, v85\n"
    "v_mfma_f32_16x16x32_bf16 v[72:75], a[232:235], a[16:19], v[72:75]\n"
    "v_cvt_pk_bf16_f32 v204, v168, v169\n"
    "v_mfma_f32_16x16x32_bf16 v[72:75], a[236:239], a[20:23], v[72:75]\n"
    "buffer_load_dwordx4 v230, s[20:23], s73 offen offset:2048 nt lds\n"
    "v_cvt_pk_bf16_f32 v205, v170, v171\n"
    "v_mfma_f32_16x16x32_bf16 v[76:79], a[216:219], a[24:27], 0\n"
    "s_add_u32 s52, s52, 5947392\n"
    "s_add_u32 s53, s53, 16\n"
    "v_mfma_f32_16x16x32_bf16 v[76:79], a[220:223], a[28:31], v[76:79]\n"
    "s_cmp_ge_u32 s53, 48\n"
    "s_cselect_b32 s66, 1032192, 0\n"
    "s_cselect_b32 s67, 48, 0\n"
    "v_mfma_f32_16x16x32_bf16 v[76:79], a[224:227], a[32:35], v[76:79]\n"
    "s_add_u32 s52, s52, s66\n"
    "s_sub_u32 s53, s53, s67\n"
    "v_mfma_f32_16x16x32_bf16 v[76:79], a[228:231], a[36:39], v[76:79]\n"
    "s_add_u32 s62, s62, 24576\n"
    "s_cmp_eq_u32 s62, 122880\n"
    "s_cselect_b32 s62, 0, s62\n"
    "v_mfma_f32_16x16x32_bf16 v[76:79], a[232:235], a[40:43], v[76:79]\n"
    "ds_write_b64 v237, v[204:205] offset:18688\n"
    "v_mfma_f32_16x16x32_bf16 v[76:79], a[236:239], a[44:47], v[76:79]\n"
    "v_cvt_pk_bf16_f32 v206, v172, v173\n"
    "v_cvt_pk_bf16_f32 v207, v174, v175\n"
    "v_mfma_f32_16x16x32_bf16 v[8:11], a[144:147], a[48:51], 0\n"
    "ds_write_b64 v237, v[206:207] offset:18696\n"
    "v_mfma_f32_16x16x32_bf16 v[8:11], a[148:151], a[52:55], v[8:11]\n"
    "v_cvt_pk_bf16_f32 v127, v86, v87\n"
    "v_cvt_pk_bf16_f32 v128, v16, v17\n"
    "v_mfma_f32_16x16x32_bf16 v[8:11], a[152:155], a[56:59], v[8:11]\n"
    "v_cvt_pk_bf16_f32 v129, v18, v19\n"
    "v_mfma_f32_16x16x32_bf16 v[8:11], a[156:159], a[60:63], v[8:11]\n"
    "v_cvt_pk_bf16_f32 v136, v20, v21\n"
    "v_cvt_pk_bf16_f32 v137, v22, v23\n"
    "v_mfma_f32_16x16x32_bf16 v[8:11], a[160:163], a[64:67], v[8:11]\n"
    "v_cvt_pk_bf16_f32 v130, v40, v41\n"
    "v_mfma_f32_16x16x32_bf16 v[8:11], a[164:167], a[68:71], v[8:11]\n"
    "v_cvt_pk_bf16_f32 v131, v42, v43\n"
    "v_cvt_pk_bf16_f32 v138, v44, v45\n"
    "v_mfma_f32_16x16x32_bf16 v[12:15], a[144:147], a[72:75], 0\n"
    "v_cvt_pk_bf16_f32 v139, v46, v47\n"
    "v_mfma_f32_16x16x32_bf16 v[12:15], a[148:151], a[76:79], v[12:15]\n"
    "v_cvt_pk_bf16_f32 v132, v64, v65\n"
    "v_cvt_pk_bf16_f32 v133, v66, v67\n"
    "v_mfma_f32_16x16x32_bf16 v[12:15], a[152:155], a[80:83], v[12:15]\n"
    "v_cvt_pk_bf16_f32 v140, v68, v69\n"
    "v_mfma_f32_16x16x32_bf16 v[12:15], a[156:159], a[84:87], v[12:15]\n"
    "v_cvt_pk_bf16_f32 v141, v70, v71\n"
    "v_cvt_pk_bf16_f32 v134, v88, v89\n"
    "v_mfma_f32_16x16x32_bf16 v[12:15], a[160:163], a[88:91], v[12:15]\n"
    "s_waitcnt lgkmcnt(0)\n"
    "s_barrier\n"
    "v_mfma_f32_16x16x32_bf16 v[12:15], a[164:167], a[92:95], v[12:15]\n"
    "ds_read_b128 v[208:211], v239\n"
    "v_cvt_pk_bf16_f32 v135, v90, v91\n"
    "v_mfma_f32_16x16x32_bf16 v[32:35], a[168:171], a[48:51], 0\n"
    "ds_read_b128 v[212:215], v239 offset:1024\n"
    "v_mfma_f32_16x16x32_bf16 v[32:35], a[172:175], a[52:55], v[32:35]\n"
    "ds_read_b128 v[216:219], v239 offset:2048\n"
    "v_cvt_pk_bf16_f32 v142, v92, v93\n"
    "v_mfma_f32_16x16x32_bf16 v[32:35], a[176:179], a[56:59], v[32:35]\n"
    "v_cvt_pk_bf16_f32 v143, v94, v95\n"
    "v_mfma_f32_16x16x32_bf16 v[32:35], a[180:183], a[60:63], v[32:35]\n"
    "v_mfma_f32_16x16x32_bf16 v[32:35], a[184:187], a[64:67], v[32:35]\n"
    "v_mfma_f32_16x16x32_bf16 v[32:35], a[188:191], a[68:71], v[32:35]\n"
    "ds_read_b128 v[220:223], v239 offset:6144\n"
    "v_mfma_f32_16x16x32_bf16 v[36:39], a[168:171], a[72:75], 0\n"
    "v_mfma_f32_16x16x32_bf16 v[36:39], a[172:175], a[76:79], v[36:39]\n"
    "s_add_u32 s69, s54, 0\n"
    "v_mfma_f32_16x16x32_bf16 v[36:39], a[176:179], a[80:83], v[36:39]\n"
    "s_waitcnt lgkmcnt(3)\n"
    "buffer_store_dwordx4 v[208:211], v228, s[24:27], s69 offen sc1\n"
    "v_mfma_f32_16x16x32_bf16 v[36:39], a[180:183], a[84:87], v[36:39]\n"
    "s_nop 0\n"
    "ds_read_b128 v[208:211], v239 offset:7168\n"
    "v_mfma_f32_16x16x32_bf16 v[36:39], a[184:187], a[88:91], v[36:39]\n"
    "v_mfma_f32_16x16x32_bf16 v[36:39], a[188:191], a[92:95], v[36:39]\n"
    "v_mfma_f32_16x16x32_bf16 v[56:59], a[192:195], a[48:51], 0\n"
    "s_waitcnt lgkmcnt(2)\n"
    "buffer_store_dwordx4 v[212:215], v229, s[24:27], s69 offen offset:1024 sc1\n"
    "v_mfma_f32_16x16x32_bf16 v[56:59], a[196:199], a[52:55], v[56:59]\n"
    "s_nop 0\n"
    "ds_read_b128 v[212:215], v239 offset:8192\n"
    "v_mfma_f32_16x16x32_bf16 v[56:59], a[200:203], a[56:59], v[56:59]\n"
    "v_mfma_f32_16x16x32_bf16 v[56:59], a[204:207], a[60:63], v[56:59]\n"
    "v_mfma_f32_16x16x32_bf16 v[56:59], a[208:211], a[64:67], v[56:59]\n"
    "buffer_store_dwordx4 v[216:219], v230, s[24:27], s69 offen offset:2048 sc1\n"
    "v_mfma_f32_16x16x32_bf16 v[56:59], a[212:215], a[68:71], v[56:59]\n"
    "s_nop 0\n"
    "ds_read_b128 v[216:219], v239 offset:12288\n"
    "v_mfma_f32_16x16x32_bf16 v[60:63], a[192:195], a[72:75], 0\n"
    "v_mfma_f32_16x16x32_bf16 v[60:63], a[196:199], a[76:79], v[60:63]\n"
    "s_add_u32 s69, s54, 294912\n"
    "v_mfma_f32_16x16x32_bf16 v[60:63], a[200:203], a[80:83], v[60:63]\n"
    "s_waitcnt lgkmcnt(2)\n"
    "buffer_store_dwordx4 v[220:223], v228, s[24:27], s69 offen sc1\n"
    "v_mfma_f32_16x16x32_bf16 v[60:63], a[204:207], a[84:87], v[60:63]\n"
    "s_nop 0\n"
    "ds_read_b128 v[220:223], v239 offset:13312\n"
    "v_mfma_f32_16x16x32_bf16 v[60:63], a[208:211], a[88:91], v[60:63]\n"
    "v_mfma_f32_16x16x32_bf16 v[60:63], a[212:215], a[92:95], v[60:63]\n"
    "v_mfma_f32_16x16x32_bf16 v[80:83], a[216:219], a[48:51], 0\n"
    "buffer_store_dwordx4 v[208:211], v229, s[24:27], s69 offen offset:1024 sc1\n"
    "v_mfma_f32_16x16x32_bf16 v[80:83], a[220:223], a[52:55], v[80:83]\n"
    "s_nop 0\n"
    "ds_read_b128 v[208:211], v239 offset:14336\n"
    "v_mfma_f32_16x16x32_bf16 v[80:83], a[224:227], a[56:59], v[80:83]\n"
    "v_mfma_f32_16x16x32_bf16 v[80:83], a[228:231], a[60:63], v[80:83]\n"
    "v_mfma_f32_16x16x32_bf16 v[80:83], a[232:235], a[64:67], v[80:83]\n"
    "s_waitcnt lgkmcnt(2)\n"
    "buffer_store_dwordx4 v[212:215], v230, s[24:27], s69 offen offset:2048 sc1\n"
    "v_mfma_f32_16x16x32_bf16 v[80:83], a[236:239], a[68:71], v[80:83]\n"
    "v_mfma_f32_16x16x32_bf16 v[84:87], a[216:219], a[72:75], 0\n"
    "v_mfma_f32_16x16x32_bf16 v[84:87], a[220:223], a[76:79], v[84:87]\n"
    "s_add_u32 s69, s54, 589824\n"
    "v_mfma_f32_16x16x32_bf16 v[84:87], a[224:227], a[80:83], v[84:87]\n"
    "buffer_store_dwordx4 v[216:219], v228, s[24:27], s69 offen sc1\n"
    "v_mfma_f32_16x16x32_bf16 v[84:87], a[228:231], a[84:87], v[84:87]\n"
    "v_mfma_f32_16x16x32_bf16 v[84:87], a[232:235], a[88:91], v[84:87]\n"
    "v_mfma_f32_16x16x32_bf16 v[84:87], a[236:239], a[92:95], v[84:87]\n"
    "v_mfma_f32_16x16x32_bf16 v[16:19], a[144:147], a[96:99], 0\n"
    "s_waitcnt lgkmcnt(0)\n"
    "buffer_store_dwordx4 v[220:223], v229, s[24:27], s69 offen offset:1024 sc1\n"
    "v_mfma_f32_16x16x32_bf16 v[16:19], a[148:151], a[100:103], v[16:19]\n"
    "v_mfma_f32_16x16x32_bf16 v[16:19], a[152:155], a[104:107], v[16:19]\n"
    "v_mfma_f32_16x16x32_bf16 v[16:19], a[156:159], a[108:111], v[16:19]\n"
    "v_mfma_f32_16x16x32_bf16 v[16:19], a[160:163], a[112:115], v[16:19]\n"
    "buffer_store_dwordx4 v[208:211], v230, s[24:27], s69 offen offset:2048 sc1\n"
    "v_mfma_f32_16x16x32_bf16 v[16:19], a[164:167], a[116:119], v[16:19]\n"
    "s_add_u32 s54, s54, 5947392\n"
    "s_add_u32 s55, s55, 16\n"
    "v_mfma_f32_16x16x32_bf16 v[20:23], a[144:147], a[120:123], 0\n"
    "s_cmp_ge_u32 s55, 48\n"
    "s_cselect_b32 s66, 1032192, 0\n"
    "s_cselect_b32 s67, 48, 0\n"
    "v_mfma_f32_16x16x32_bf16 v[20:23], a[148:151], a[124:127], v[20:23]\n"
    "s_add_u32 s54, s54, s66\n"
    "s_sub_u32 s55, s55, s67\n"
    "v_mfma_f32_16x16x32_bf16 v[20:23], a[152:155], a[128:131], v[20:23]\n"
    "v_mfma_f32_16x16x32_bf16 v[20:23], a[156:159], a[132:135], v[20:23]\n"
    "v_mfma_f32_16x16x32_bf16 v[20:23], a[160:163], a[136:139], v[20:23]\n"
    "v_mfma_f32_16x16x32_bf16 v[20:23], a[164:167], a[140:143], v[20:23]\n"
    "v_mfma_f32_16x16x32_bf16 v[40:43], a[168:171], a[96:99], 0\n"
    "v_mfma_f32_16x16x32_bf16 v[40:43], a[172:175], a[100:103], v[40:43]\n"
    "v_mfma_f32_16x16x32_bf16 v[40:43], a[176:179], a[104:107], v[40:43]\n"
    "v_mfma_f32_16x16x32_bf16 v[40:43], a[180:183], a[108:111], v[40:43]\n"
    "v_mfma_f32_16x16x32_bf16 v[40:43], a[184:187], a[112:115], v[40:43]\n"
    "v_mfma_f32_16x16x32_bf16 v[40:43], a[188:191], a[116:119], v[40:43]\n"
    "v_mfma_f32_16x16x32_bf16 v[44:47], a[168:171], a[120:123], 0\n"
    "v_mfma_f32_16x16x32_bf16 v[44:47], a[172:175], a[124:127], v[44:47]\n"
    "v_mfma_f32_16x16x32_bf16 v[44:47], a[176:179], a[128:131], v[44:47]\n"
    "v_mfma_f32_16x16x32_bf16 v[44:47], a[180:183], a[132:135], v[44:47]\n"
    "v_mfma_f32_16x16x32_bf16 v[44:47], a[184:187], a[136:139], v[44:47]\n"
    "v_mfma_f32_16x16x32_bf16 v[44:47], a[188:191], a[140:143], v[44:47]\n"
    "s_cmp_ge_u32 s60, 4\n"
    "s_cbranch_scc1 .Lw_w60\n"
    "s_waitcnt vmcnt(33)\n"
    ".Lw_w60:\n"
    "s_waitcnt vmcnt(51)\n"
    "s_add_u32 s60, s60, 1\n"
    "s_waitcnt lgkmcnt(0)\n"
    "s_barrier\n"
    "s_branch .Lw_B1\n"
    ".Lw_EPIB:\n"
    "v_mfma_f32_16x16x32_bf16 v[144:147], v[112:115], a[240:243], 0\n"
    "v_cvt_pk_bf16_f32 v204, v168, v169\n"
    "v_cvt_pk_bf16_f32 v205, v170, v171\n"
    "v_mfma_f32_16x16x32_bf16 v[144:147], v[116:119], a[244:247], v[144:147]\n"
    "ds_write_b64 v237, v[204:205] offset:18432\n"
    "v_mfma_f32_16x16x32_bf16 v[148:151], v[120:123], a[240:243], 0\n"
    "v_cvt_pk_bf16_f32 v206, v172, v173\n"
    "v_cvt_pk_bf16_f32 v207, v174, v175\n"
    "v_mfma_f32_16x16x32_bf16 v[148:151], v[124:127], a[244:247], v[148:151]\n"
    "ds_write_b64 v237, v[206:207] offset:18440\n"
    "v_mfma_f32_16x16x32_bf16 v[152:155], v[112:115], a[248:251], 0\n"
    "v_mfma_f32_16x16x32_bf16 v[152:155], v[116:119], a[252:255], v[152:155]\n"
    "v_mfma_f32_16x16x32_bf16 v[156:159], v[120:123], a[248:251], 0\n"
    "v_mfma_f32_16x16x32_bf16 v[156:159], v[124:127], a[252:255], v[156:159]\n"
    "v_cvt_pk_bf16_f32 v192, v144, v145\n"
    "v_cvt_pk_bf16_f32 v193, v146, v147\n"
    "v_mfma_f32_16x16x32_bf16 v[160:163], v[112:115], v[176:179], 0\n"
    "ds_write_b64 v237, v[192:193] offset:128\n"
    "v_mfma_f32_16x16x32_bf16 v[160:163], v[116:119], v[180:183], v[160:163]\n"
    "v_cvt_pk_bf16_f32 v194, v148, v149\n"
    "v_cvt_pk_bf16_f32 v195, v150, v151\n"
    "v_mfma_f32_16x16x32_bf16 v[164:167], v[120:123], v[176:179], 0\n"
    "ds_write_b64 v237, v[194:195] offset:136\n"
    "v_mfma_f32_16x16x32_bf16 v[164:167], v[124:127], v[180:183], v[164:167]\n"
    "v_cvt_pk_bf16_f32 v196, v152, v153\n"
    "v_cvt_pk_bf16_f32 v197, v154, v155\n"
    "v_mfma_f32_16x16x32_bf16 v[168:171], v[112:115], v[184:187], 0\n"
    "ds_write_b64 v237, v[196:197] offset:6272\n"
    "v_mfma_f32_16x16x32_bf16 v[168:171], v[116:119], v[188:191], v[168:171]\n"
    "v_cvt_pk_bf16_f32 v198, v156, v157\n"
    "v_cvt_pk_bf16_f32 v199, v158, v159\n"
    "v_mfma_f32_16x16x32_bf16 v[172:175], v[120:123], v[184:187], 0\n"
    "ds_write_b64 v237, v[198:199] offset:6280\n"
    "v_mfma_f32_16x16x32_bf16 v[172:175], v[124:127], v[188:191], v[172:175]\n"
    "v_cvt_pk_bf16_f32 v200, v160, v161\n"
    "v_cvt_pk_bf16_f32 v201, v162, v163\n"
    "v_mfma_f32_16x16x32_bf16 v[144:147], v[128:131], a[240:243], 0\n"
    "ds_write_b64 v237, v[200:201] offset:12416\n"
    "v_mfma_f32_16x16x32_bf16 v[144:147], v[132:135], a[244:247], v[144:147]\n"
    "v_cvt_pk_bf16_f32 v202, v164, v165\n"
    "v_cvt_pk_bf16_f32 v203, v166, v167\n"
    "v_mfma_f32_16x16x32_bf16 v[148:151], v[136:139], a[240:243], 0\n"
    "ds_write_b64 v237, v[202:203] offset:12424\n"
    "v_mfma_f32_16x16x32_bf16 v[148:151], v[140:143], a[244:247], v[148:151]\n"
    "v_cvt_pk_bf16_f32 v204, v168, v169\n"
    "v_cvt_pk_bf16_f32 v205, v170, v171\n"
    "v_mfma_f32_16x16x32_bf16 v[152:155], v[128:131], a[248:251], 0\n"
    "ds_write_b64 v237, v[204:205] offset:18560\n"
    "v_mfma_f32_16x16x32_bf16 v[152:155], v[132:135], a[252:255], v[152:155]\n"
    "v_cvt_pk_bf16_f32 v206, v172, v173\n"
    "v_cvt_pk_bf16_f32 v207, v174, v175\n"
    "v_mfma_f32_16x16x32_bf16 v[156:159], v[136:139], a[248:251], 0\n"
    "ds_write_b64 v237, v[206:207] offset:18568\n"
    "v_mfma_f32_16x16x32_bf16 v[156:159], v[140:143], a[252:255], v[156:159]\n"
    "v_cvt_pk_bf16_f32 v192, v144, v145\n"
    "v_cvt_pk_bf16_f32 v193, v146, v147\n"
    "v_mfma_f32_16x16x32_bf16 v[160:163], v[128:131], v[176:179], 0\n"
    "ds_write_b64 v237, v[192:193] offset:256\n"
    "v_mfma_f32_16x16x32_bf16 v[160:163], v[132:135], v[180:183], v[160:163]\n"
    "v_cvt_pk_bf16_f32 v194, v148, v149\n"
    "v_cvt_pk_bf16_f32 v195, v150, v151\n"
    "v_mfma_f32_16x16x32_bf16 v[164:167], v[136:139], v[176:179], 0\n"
    "ds_write_b64 v237, v[194:195] offset:264\n"
    "v_mfma_f32_16x16x32_bf16 v[164:167], v[140:143], v[180:183], v[164:167]\n"
    "v_cvt_pk_bf16_f32 v196, v152, v153\n"
    "v_cvt_pk_bf16_f32 v197, v154, v155\n"
    "v_mfma_f32_16x16x32_bf16 v[168:171], v[128:131], v[184:187], 0\n"
    "ds_write_b64 v237, v[196:197] offset:6400\n"
    "v_mfma_f32_16x16x32_bf16 v[168:171], v[132:135], v[188:191], v[168:171]\n"
    "v_cvt_pk_bf16_f32 v198, v156, v157\n"
    "v_cvt_pk_bf16_f32 v199, v158, v159\n"
    "v_mfma_f32_16x16x32_bf16 v[172:175], v[136:139], v[184:187], 0\n"
    "ds_write_b64 v237, v[198:199] offset:6408\n"
    "v_mfma_f32_16x16x32_bf16 v[172:175], v[140:143], v[188:191], v[172:175]\n"
    "v_cvt_pk_bf16_f32 v200, v160, v161\n"
    "v_cvt_pk_bf16_f32 v201, v162, v163\n"
    "ds_write_b64 v237, v[200:201] offset:12544\n"
    "v_cvt_pk_bf16_f32 v202, v164, v165\n"
    "v_cvt_pk_bf16_f32 v203, v166, v167\n"
    "ds_write_b64 v237, v[202:203] offset:12552\n"
    "v_cvt_pk_bf16_f32 v204, v168, v169\n"
    "v_cvt_pk_bf16_f32 v205, v170, v171\n"
    "ds_write_b64 v237, v[204:205] offset:18688\n"
    "v_cvt_pk_bf16_f32 v206, v172, v173\n"
    "v_cvt_pk_bf16_f32 v207, v174, v175\n"
    "ds_write_b64 v237, v[206:207] offset:18696\n"
    "s_waitcnt lgkmcnt(0)\n"
    "s_barrier\n"
    "v_cvt_pk_bf16_f32 v96, v0, v1\n"
    "v_cvt_pk_bf16_f32 v97, v2, v3\n"
    "v_cvt_pk_bf16_f32 v104, v4, v5\n"
    "v_cvt_pk_bf16_f32 v105, v6, v7\n"
    "v_cvt_pk_bf16_f32 v98, v24, v25\n"
    "v_cvt_pk_bf16_f32 v99, v26, v27\n"
    "v_cvt_pk_bf16_f32 v106, v28, v29\n"
    "v_cvt_pk_bf16_f32 v107, v30, v31\n"
    "v_cvt_pk_bf16_f32 v100, v48, v49\n"
    "v_cvt_pk_bf16_f32 v101, v50, v51\n"
    "v_cvt_pk_bf16_f32 v108, v52, v53\n"
    "v_cvt_pk_bf16_f32 v109, v54, v55\n"
    "v_cvt_pk_bf16_f32 v102, v72, v73\n"
    "v_cvt_pk_bf16_f32 v103, v74, v75\n"
    "v_cvt_pk_bf16_f32 v110, v76, v77\n"
    "v_cvt_pk_bf16_f32 v111, v78, v79\n"
    "v_cvt_pk_bf16_f32 v112, v8, v9\n"
    "v_cvt_pk_bf16_f32 v113, v10, v11\n"
    "v_cvt_pk_bf16_f32 v120, v12, v13\n"
    "v_cvt_pk_bf16_f32 v121, v14, v15\n"
    "v_cvt_pk_bf16_f32 v114, v32, v33\n"
    "v_cvt_pk_bf16_f32 v115, v34, v35\n"
    "v_cvt_pk_bf16_f32 v122, v36, v37\n"
    "v_cvt_pk_bf16_f32 v123, v38, v39\n"
    "v_cvt_pk_bf16_f32 v116, v56, v57\n"
    "v_cvt_pk_bf16_f32 v117, v58, v59\n"
    "v_cvt_pk_bf16_f32 v124, v60, v61\n"
    "v_cvt_pk_bf16_f32 v125, v62, v63\n"
    "v_cvt_pk_bf16_f32 v118, v80, v81\n"
    "v_cvt_pk_bf16_f32 v119, v82, v83\n"
    "v_cvt_pk_bf16_f32 v126, v84, v85\n"
    "v_cvt_pk_bf16_f32 v127, v86, v87\n"
    "v_cvt_pk_bf16_f32 v128, v16, v17\n"
    "v_cvt_pk_bf16_f32 v129, v18, v19\n"
    "v_cvt_pk_bf16_f32 v136, v20, v21\n"
    "v_cvt_pk_bf16_f32 v137, v22, v23\n"
    "v_cvt_pk_bf16_f32 v130, v40, v41\n"
    "v_cvt_pk_bf16_f32 v131, v42, v43\n"
    "v_cvt_pk_bf16_f32 v138, v44, v45\n"
    "v_cvt_pk_bf16_f32 v139, v46, v47\n"
    "v_cvt_pk_bf16_f32 v132, v64, v65\n"
    "v_cvt_pk_bf16_f32 v133, v66, v67\n"
    "v_cvt_pk_bf16_f32 v140, v68, v69\n"
    "v_cvt_pk_bf16_f32 v141, v70, v71\n"
    "v_cvt_pk_bf16_f32 v134, v88, v89\n"
    "v_cvt_pk_bf16_f32 v135, v90, v91\n"
    "v_cvt_pk_bf16_f32 v142, v92, v93\n"
    "v_cvt_pk_bf16_f32 v143, v94, v95\n"
    "ds_read_b128 v[0:3], v239\n"
    "ds_read_b128 v[4:7], v239 offset:1024\n"
    "ds_read_b128 v[8:11], v239 offset:2048\n"
    "ds_read_b128 v[12:15], v239 offset:6144\n"
    "ds_read_b128 v[16:19], v239 offset:7168\n"
    "ds_read_b128 v[20:23], v239 offset:8192\n"
    "ds_read_b128 v[24:27], v239 offset:12288\n"
    "ds_read_b128 v[28:31], v239 offset:13312\n"
    "ds_read_b128 v[32:35], v239 offset:14336\n"
    "ds_read_b128 v[36:39], v239 offset:18432\n"
    "ds_read_b128 v[40:43], v239 offset:19456\n"
    "ds_read_b128 v[44:47], v239 offset:20480\n"
    "s_add_u32 s69, s54, 0\n"
    "s_waitcnt lgkmcnt(11)\n"
    "buffer_store_dwordx4 v[0:3], v228, s[24:27], s69 offen sc1\n"
    "s_waitcnt lgkmcnt(10)\n"
    "buffer_store_dwordx4 v[4:7], v229, s[24:27], s69 offen offset:1024 sc1\n"
    "s_waitcnt lgkmcnt(9)\n"
    "buffer_store_dwordx4 v[8:11], v230, s[24:27], s69 offen offset:2048 sc1\n"
    "s_add_u32 s69, s54, 294912\n"
    "s_waitcnt lgkmcnt(8)\n"
    "buffer_store_dwordx4 v[12:15], v228, s[24:27], s69 offen sc1\n"
    "s_waitcnt lgkmcnt(7)\n"
    "buffer_store_dwordx4 v[16:19], v229, s[24:27], s69 offen offset:1024 sc1\n"
    "s_waitcnt lgkmcnt(6)\n"
    "buffer_store_dwordx4 v[20:23], v230, s[24:27], s69 offen offset:2048 sc1\n"
    "s_add_u32 s69, s54, 589824\n"
    "s_waitcnt lgkmcnt(5)\n"
    "buffer_store_dwordx4 v[24:27], v228, s[24:27], s69 offen sc1\n"
    "s_waitcnt lgkmcnt(4)\n"
    "buffer_store_dwordx4 v[28:31], v229, s[24:27], s69 offen offset:1024 sc1\n"
    "s_waitcnt lgkmcnt(3)\n"
    "buffer_store_dwordx4 v[32:35], v230, s[24:27], s69 offen offset:2048 sc1\n"
    "s_add_u32 s69, s54, 884736\n"
    "s_waitcnt lgkmcnt(2)\n"
    "buffer_store_dwordx4 v[36:39], v228, s[24:27], s69 offen sc1\n"
    "s_waitcnt lgkmcnt(1)\n"
    "buffer_store_dwordx4 v[40:43], v229, s[24:27], s69 offen offset:1024 sc1\n"
    "s_waitcnt lgkmcnt(0)\n"
    "buffer_store_dwordx4 v[44:47], v230, s[24:27], s69 offen offset:2048 sc1\n"
    "s_mov_b32 s64, s63\n"
    "s_mov_b32 s63, s61\n"
    "s_add_u32 s61, s61, 24576\n"
    "s_cmp_eq_u32 s61, 122880\n"
    "s_cselect_b32 s61, 0, s61\n"
    "s_sub_u32 s63, s61, s63\n"
    "v_add_u32 v237, s64, v237\n"
    "v_add_u32 v239, s64, v239\n"
    "s_add_u32 s60, s60, 1\n"
    "s_add_u32 s57, s57, 589824\n"
    "v_add_u32 v240, 12288, v240\n"
    "s_waitcnt lgkmcnt(0)\n"
    "s_barrier\n"
    "ds_read_b128 a[240:243], v238\n"
    "ds_read_b128 a[244:247], v238 offset:1024\n"
    "ds_read_b128 a[248:251], v238 offset:2048\n"
    "ds_read_b128 a[252:255], v238 offset:3072\n"
    "ds_read_b128 v[176:179], v238 offset:4096\n"
    "ds_read_b128 v[180:183], v238 offset:5120\n"
    "ds_read_b128 v[184:187], v238 offset:6144\n"
    "ds_read_b128 v[188:191], v238 offset:7168\n"
    "s_waitcnt lgkmcnt(7)\n"
    "v_mfma_f32_16x16x32_bf16 v[144:147], v[96:99], a[240:243], 0\n"
    "s_waitcnt lgkmcnt(6)\n"
    "v_mfma_f32_16x16x32_bf16 v[144:147], v[100:103], a[244:247], v[144:147]\n"
    "v_mfma_f32_16x16x32_bf16 v[148:151], v[104:107], a[240:243], 0\n"
    "v_mfma_f32_16x16x32_bf16 v[148:151], v[108:111], a[244:247], v[148:151]\n"
    "v_mfma_f32_16x16x32_bf16 v[152:155], v[112:115], a[240:243], 0\n"
    "v_mfma_f32_16x16x32_bf16 v[152:155], v[116:119], a[244:247], v[152:155]\n"
    "v_mfma_f32_16x16x32_bf16 v[156:159], v[120:123], a[240:243], 0\n"
    "v_mfma_f32_16x16x32_bf16 v[156:159], v[124:127], a[244:247], v[156:159]\n"
    "s_nop 1\n"
    "v_cvt_pk_bf16_f32 v192, v144, v145\n"
    "v_cvt_pk_bf16_f32 v193, v146, v147\n"
    "v_mfma_f32_16x16x32_bf16 v[160:163], v[128:131], a[240:243], 0\n"
    "v_cvt_pk_bf16_f32 v194, v148, v149\n"
    "v_cvt_pk_bf16_f32 v195, v150, v151\n"
    "v_mfma_f32_16x16x32_bf16 v[160:163], v[132:135], a[244:247], v[160:163]\n"
    "ds_write_b128 v237, v[192:195]\n"
    "v_mfma_f32_16x16x32_bf16 v[164:167], v[136:139], a[240:243], 0\n"
    "v_mfma_f32_16x16x32_bf16 v[164:167], v[140:143], a[244:247], v[164:167]\n"
    "v_cvt_pk_bf16_f32 v196, v152, v153\n"
    "v_cvt_pk_bf16_f32 v197, v154, v155\n"
    "s_waitcnt lgkmcnt(1)\n"
    "v_mfma_f32_16x16x32_bf16 v[168:171], v[96:99], a[248:251], 0\n"
    "v_cvt_pk_bf16_f32 v198, v156, v157\n"
    "v_cvt_pk_bf16_f32 v199, v158, v159\n"
    "v_mfma_f32_16x16x32_bf16 v[168:171], v[100:103], a[252:255], v[168:171]\n"
    "ds_write_b128 v237, v[196:199] offset:128\n"
    "v_mfma_f32_16x16x32_bf16 v[172:175], v[104:107], a[248:251], 0\n"
    "v_mfma_f32_16x16x32_bf16 v[172:175], v[108:111], a[252:255], v[172:175]\n"
    "v_cvt_pk_bf16_f32 v200, v160, v161\n"
    "v_cvt_pk_bf16_f32 v201, v162, v163\n"
    "v_mfma_f32_16x16x32_bf16 v[144:147], v[112:115], a[248:251], 0\n"
    "v_cvt_pk_bf16_f32 v202, v164, v165\n"
    "v_cvt_pk_bf16_f32 v203, v166, v167\n"
    "v_mfma_f32_16x16x32_bf16 v[144:147], v[116:119], a[252:255], v[144:147]\n"
    "ds_write_b128 v237, v[200:203] offset:256\n"
    "v_mfma_f32_16x16x32_bf16 v[148:151], v[120:123], a[248:251], 0\n"
    "v_mfma_f32_16x16x32_bf16 v[148:151], v[124:127], a[252:255], v[148:151]\n"
    "v_cvt_pk_bf16_f32 v204, v168, v169\n"
    "v_cvt_pk_bf16_f32 v205, v170, v171\n"
    "v_mfma_f32_16x16x32_bf16 v[152:155], v[128:131], a[248:251], 0\n"
    "v_cvt_pk_bf16_f32 v206, v172, v173\n"
    "v_cvt_pk_bf16_f32 v207, v174, v175\n"
    "v_mfma_f32_16x16x32_bf16 v[152:155], v[132:135], a[252:255], v[152:155]\n"
    "ds_write_b128 v237, v[204:207] offset:6144\n"
    "v_mfma_f32_16x16x32_bf16 v[156:159], v[136:139], a[248:251], 0\n"
    "v_mfma_f32_16x16x32_bf16 v[156:159], v[140:143], a[252:255], v[156:159]\n"
    "v_cvt_pk_bf16_f32 v192, v144, v145\n"
    "v_cvt_pk_bf16_f32 v193, v146, v147\n"
    "v_cvt_pk_bf16_f32 v194, v148, v149\n"
    "v_cvt_pk_bf16_f32 v195, v150, v151\n"
    "ds_write_b128 v237, v[192:195] offset:6272\n"
    "v_cvt_pk_bf16_f32 v196, v152, v153\n"
    "v_cvt_pk_bf16_f32 v197, v154, v155\n"
    "s_nop 0\n"
    "v_cvt_pk_bf16_f32 v198, v156, v157\n"
    "v_cvt_pk_bf16_f32 v199, v158, v159\n"
    "ds_write_b128 v237, v[196:199] offset:6400\n"
    "s_waitcnt lgkmcnt(0)\n"
    "s_barrier\n"
    "v_mfma_f32_16x16x32_bf16 v[160:163], v[96:99], v[176:179], 0\n"
    "v_mfma_f32_16x16x32_bf16 v[160:163], v[100:103], v[180:183], v[160:163]\n"
    "v_mfma_f32_16x16x32_bf16 v[164:167], v[104:107], v[176:179], 0\n"
    "v_mfma_f32_16x16x32_bf16 v[164:167], v[108:111], v[180:183], v[164:167]\n"
    "v_mfma_f32_16x16x32_bf16 v[168:171], v[112:115], v[176:179], 0\n"
    "v_mfma_f32_16x16x32_bf16 v[168:171], v[116:119], v[180:183], v[168:171]\n"
    "v_mfma_f32_16x16x32_bf16 v[172:175], v[120:123], v[176:179], 0\n"
    "v_mfma_f32_16x16x32_bf16 v[172:175], v[124:127], v[180:183], v[172:175]\n"
    "s_nop 1\n"
    "v_cvt_pk_bf16_f32 v192, v160, v161\n"
    "v_cvt_pk_bf16_f32 v193, v162, v163\n"
    "v_mfma_f32_16x16x32_bf16 v[144:147], v[128:131], v[176:179], 0\n"
    "v_cvt_pk_bf16_f32 v194, v164, v165\n"
    "v_cvt_pk_bf16_f32 v195, v166, v167\n"
    "v_mfma_f32_16x16x32_bf16 v[144:147], v[132:135], v[180:183], v[144:147]\n"
    "ds_write_b128 v237, v[192:195] offset:12288\n"
    "v_mfma_f32_16x16x32_bf16 v[148:151], v[136:139], v[176:179], 0\n"
    "v_mfma_f32_16x16x32_bf16 v[148:151], v[140:143], v[180:183], v[148:151]\n"
    "v_cvt_pk_bf16_f32 v196, v168, v169\n"
    "v_cvt_pk_bf16_f32 v197, v170, v171\n"
    "v_mfma_f32_16x16x32_bf16 v[152:155], v[96:99], v[184:187], 0\n"
    "v_cvt_pk_bf16_f32 v198, v172, v173\n"
    "v_cvt_pk_bf16_f32 v199, v174, v175\n"
    "v_mfma_f32_16x16x32_bf16 v[152:155], v[100:103], v[188:191], v[152:155]\n"
    "ds_write_b128 v237, v[196:199] offset:12416\n"
    "v_mfma_f32_16x16x32_bf16 v[156:159], v[104:107], v[184:187], 0\n"
    "v_mfma_f32_16x16x32_bf16 v[156:159], v[108:111], v[188:191], v[156:159]\n"
    "v_cvt_pk_bf16_f32 v200, v144, v145\n"
    "v_cvt_pk_bf16_f32 v201, v146, v147\n"
    "v_mfma_f32_16x16x32_bf16 v[160:163], v[112:115], v[184:187], 0\n"
    "v_cvt_pk_bf16_f32 v202, v148, v149\n"
    "v_cvt_pk_bf16_f32 v203, v150, v151\n"
    "v_mfma_f32_16x16x32_bf16 v[160:163], v[116:119], v[188:191], v[160:163]\n"
    "ds_write_b128 v237, v[200:203] offset:12544\n"
    "v_mfma_f32_16x16x32_bf16 v[164:167], v[120:123], v[184:187], 0\n"
    "v_mfma_f32_16x16x32_bf16 v[164:167], v[124:127], v[188:191], v[164:167]\n"
    "v_cvt_pk_bf16_f32 v204, v152, v153\n"
    "v_cvt_pk_bf16_f32 v205, v154, v155\n"
    "v_mfma_f32_16x16x32_bf16 v[168:171], v[128:131], v[184:187], 0\n"
    "v_cvt_pk_bf16_f32 v206, v156, v157\n"
    "v_cvt_pk_bf16_f32 v207, v158, v159\n"
    "v_mfma_f32_16x16x32_bf16 v[168:171], v[132:135], v[188:191], v[168:171]\n"
    "ds_write_b128 v237, v[204:207] offset:18432\n"
    "v_mfma_f32_16x16x32_bf16 v[172:175], v[136:139], v[184:187], 0\n"
    "v_mfma_f32_16x16x32_bf16 v[172:175], v[140:143], v[188:191], v[172:175]\n"
    "v_cvt_pk_bf16_f32 v192, v160, v161\n"
    "v_cvt_pk_bf16_f32 v193, v162, v163\n"
    "v_cvt_pk_bf16_f32 v194, v164, v165\n"
    "v_cvt_pk_bf16_f32 v195, v166, v167\n"
    "ds_write_b128 v237, v[192:195] offset:18560\n"
    "v_cvt_pk_bf16_f32 v196, v168, v169\n"
    "v_cvt_pk_bf16_f32 v197, v170, v171\n"
    "s_nop 0\n"
    "v_cvt_pk_bf16_f32 v198, v172, v173\n"
    "v_cvt_pk_bf16_f32 v199, v174, v175\n"
    "ds_write_b128 v237, v[196:199] offset:18688\n"
    "s_waitcnt lgkmcnt(0)\n"
    "s_barrier\n"
    "ds_read_b128 v[0:3], v240\n"
    "ds_read_b128 v[4:7], v240 offset:1024\n"
    "ds_read_b128 v[8:11], v240 offset:2048\n"
    "s_add_u32 s69, s57, 0\n"
    "s_waitcnt lgkmcnt(2)\n"
    "buffer_store_dwordx4 v[0:3], v228, s[24:27], s69 offen sc1\n"
    "s_waitcnt lgkmcnt(1)\n"
    "buffer_store_dwordx4 v[4:7], v229, s[24:27], s69 offen offset:1024 sc1\n"
    "s_waitcnt lgkmcnt(0)\n"
    "buffer_store_dwordx4 v[8:11], v230, s[24:27], s69 offen offset:2048 sc1\n"
    "s_branch .Lswin_end\n"
    ".Lswin_end:\n"
    :: "{v0}"(threadIdx.x), "{s2}"(blockIdx.x), "{s[0:1]}"(__builtin_amdgcn_kernarg_segment_ptr())
    : "v1", "v2", "v3", "v4", "v5", "v6", "v7", "v8", "v9", "v10", "v11", "v12", "v13", "v14", "v15", "v16", "v17", "v18", "v19", "v20", "v21", "v22", "v23", "v24", "v25", "v26", "v27", "v28", "v29", "v30", "v31", "v32", "v33", "v34", "v35", "v36", "v37", "v38", "v39", "v40", "v41", "v42", "v43", "v44", "v45", "v46", "v47", "v48", "v49", "v50", "v51", "v52", "v53", "v54", "v55", "v56", "v57", "v58", "v59", "v60", "v61", "v62", "v63", "v64", "v65", "v66", "v67", "v68", "v69", "v70", "v71", "v72", "v73", "v74", "v75", "v76", "v77", "v78", "v79", "v80", "v81", "v82", "v83", "v84", "v85", "v86", "v87", "v88", "v89", "v90", "v91", "v92", "v93", "v94", "v95", "v96", "v97", "v98", "v99", "v100", "v101", "v102", "v103", "v104", "v105", "v106", "v107", "v108", "v109", "v110", "v111", "v112", "v113", "v114", "v115", "v116", "v117", "v118", "v119", "v120", "v121", "v122", "v123", "v124", "v125", "v126", "v127", "v128", "v129", "v130", "v131", "v132", "v133", "v134", "v135", "v136", "v137", "v138", "v139", "v140", "v141", "v142", "v143", "v144", "v145", "v146", "v147", "v148", "v149", "v150", "v151", "v152", "v153", "v154", "v155", "v156", "v157", "v158", "v159", "v160", "v161", "v162", "v163", "v164", "v165", "v166", "v167", "v168", "v169", "v170", "v171", "v172", "v173", "v174", "v175", "v176", "v177", "v178", "v179", "v180", "v181", "v182", "v183", "v184", "v185", "v186", "v187", "v188", "v189", "v190", "v191", "v192", "v193", "v194", "v195", "v196", "v197", "v198", "v199", "v200", "v201", "v202", "v203", "v204", "v205", "v206", "v207", "v208", "v209", "v210", "v211", "v212", "v213", "v214", "v215", "v216", "v217", "v218", "v219", "v220", "v221", "v222", "v223", "v224", "v225", "v226", "v227", "v228", "v229", "v230", "v231", "v232", "v233", "v234", "v235", "v236", "v237", "v238", "v239", "v240", "v241", "v242", "v243", "v244", "v245", "v246", "v247", "v248", "v249", "v250", "v251", "v252", "v253", "v254", "v255", "a0", "a1", "a2", "a3", "a4", "a5", "a6", "a7", "a8", "a9", "a10", "a11", "a12", "a13", "a14", "a15", "a16", "a17", "a18", "a19", "a20", "a21", "a22", "a23", "a24", "a25", "a26", "a27", "a28", "a29", "a30", "a31", "a32", "a33", "a34", "a35", "a36", "a37", "a38", "a39", "a40", "a41", "a42", "a43", "a44", "a45", "a46", "a47", "a48", "a49", "a50", "a51", "a52", "a53", "a54", "a55", "a56", "a57", "a58", "a59", "a60", "a61", "a62", "a63", "a64", "a65", "a66", "a67", "a68", "a69", "a70", "a71", "a72", "a73", "a74", "a75", "a76", "a77", "a78", "a79", "a80", "a81", "a82", "a83", "a84", "a85", "a86", "a87", "a88", "a89", "a90", "a91", "a92", "a93", "a94", "a95", "a96", "a97", "a98", "a99", "a100", "a101", "a102", "a103", "a104", "a105", "a106", "a107", "a108", "a109", "a110", "a111", "a112", "a113", "a114", "a115", "a116", "a117", "a118", "a119", "a120", "a121", "a122", "a123", "a124", "a125", "a126", "a127", "a128", "a129", "a130", "a131", "a132", "a133", "a134", "a135", "a136", "a137", "a138", "a139", "a140", "a141", "a142", "a143", "a144", "a145", "a146", "a147", "a148", "a149", "a150", "a151", "a152", "a153", "a154", "a155", "a156", "a157", "a158", "a159", "a160", "a161", "a162", "a163", "a164", "a165", "a166", "a167", "a168", "a169", "a170", "a171", "a172", "a173", "a174", "a175", "a176", "a177", "a178", "a179", "a180", "a181", "a182", "a183", "a184", "a185", "a186", "a187", "a188", "a189", "a190", "a191", "a192", "a193", "a194", "a195", "a196", "a197", "a198", "a199", "a200", "a201", "a202", "a203", "a204", "a205", "a206", "a207", "a208", "a209", "a210", "a211", "a212", "a213", "a214", "a215", "a216", "a217", "a218", "a219", "a220", "a221", "a222", "a223", "a224", "a225", "a226", "a227", "a228", "a229", "a230", "a231", "a232", "a233", "a234", "a235", "a236", "a237", "a238", "a239", "a240", "a241", "a242", "a243", "a244", "a245", "a246", "a247", "a248", "a249", "a250", "a251", "a252", "a253", "a254", "a255", "s3", "s4", "s5", "s6", "s7", "s8", "s9", "s10", "s11", "s12", "s13", "s14", "s15", "s16", "s17", "s18", "s19", "s20", "s21", "s22", "s23", "s24", "s25", "s26", "s27", "s28", "s29", "s30", "s31", "s32", "s33", "s34", "s35", "s36", "s37", "s38", "s39", "s40", "s41", "s42", "s43", "s44", "s45", "s46", "s47", "s48", "s49", "s50", "s51", "s52", "s53", "s54", "s55", "s56", "s57", "s58", "s59", "s60", "s61", "s62", "s63", "s64", "s65", "s66", "s67", "s68", "s69", "s70", "s71", "s72", "s73", "s74", "s75", "s76", "s77", "s78", "s79", "s80", "s81", "s82", "s83", "s84", "s85", "s86", "s87", "s88", "s89", "s90", "s91", "s92", "s93", "s94", "s95", "vcc", "scc", "memory");
}

extern "C" void kernel_launch(void* const* d_in, const int* in_sizes, int n_in,
                              void* d_out, int out_size, void* d_ws, size_t ws_size,
                              hipStream_t stream) {
  (void)hipFuncSetAttribute((const void*)swin_fused, hipFuncAttributeMaxDynamicSharedMemorySize, LDS_BYTES);
  swin_fused<<<256, 256, LDS_BYTES, stream>>>(d_in[0], d_in[1], d_in[2], d_out, d_ws);
}
```
